```python
import torch

def _softmax(x, axis=-1):
    return torch.softmax(x, dim=axis)
def _gelu(x):
    return torch.nn.functional.gelu(x, approximate="tanh")
def _silu(x):
    return torch.nn.functional.silu(x)
def _sigmoid(x):
    return torch.sigmoid(x)
def _layernorm(x, axis=-1, eps=1e-5):
    m = x.mean(dim=axis, keepdim=True)
    v = x.var(dim=axis, keepdim=True, unbiased=False)
    return (x - m) / torch.sqrt(v + eps)
def _scatter_add(tgt, idx, src, axis=0):
    return tgt.clone().index_add_(axis, idx, src)
def _segment_sum(src, seg, n):
    out = torch.zeros((n,) + src.shape[1:], dtype=src.dtype)
    return out.index_add_(0, seg, src)
def _pad(x, pw):
    flat = tuple(v for pair in reversed(pw) for v in pair)
    return torch.nn.functional.pad(x, flat)

kernel_name = 'concat_heads__grouped_kv_mqa__ssm_conv_s4d__prompt'

def setup_inputs():

    _w = (lambda a: (lambda t: t.to(torch.bfloat16) if t.is_floating_point() else t)(torch.as_tensor(a)))
    def _A_setup():
        B, S, D, H = 1, 2048, 448, 16
        Dh = D // H
        return {
            "x": _w((lambda *s: torch.randn(*s, dtype=torch.bfloat16) * 0.05)(B, S, D)),
            "wq": _w((lambda *s: torch.randn(*s, dtype=torch.bfloat16) * 1.6)(D, H * Dh)),
            "wk": _w((lambda *s: torch.randn(*s, dtype=torch.bfloat16) * 1.6)(D, Dh)),
            "wv": _w((lambda *s: torch.randn(*s, dtype=torch.bfloat16) * 0.05)(D, Dh)),
            "wo": _w((lambda *s: torch.randn(*s, dtype=torch.bfloat16) * 0.05)(H * Dh, D)),
        }
    _A_in = _A_setup()

    def _B_setup():
        Bsz, L, D, N = 1, 2048, 448, 256
        return {
            "u": _w((lambda *s: torch.randn(*s, dtype=torch.bfloat16) * 0.05)(Bsz, L, D)),
            "log_A_real": _w((lambda *s: torch.randn(*s, dtype=torch.bfloat16) * 3.2)(D, N) * 0.5),
            "A_imag": _w((lambda *s: torch.randn(*s, dtype=torch.bfloat16) * 0.05)(D, N) * 3.14),
            "C_real": _w((lambda *s: torch.randn(*s, dtype=torch.bfloat16) * 0.8)(D, N) / N ** 0.5),
            "C_imag": _w((lambda *s: torch.randn(*s, dtype=torch.bfloat16) * 0.8)(D, N) / N ** 0.5),
            "log_dt": _w((lambda *s: torch.randn(*s, dtype=torch.bfloat16) * 0.05)(D) * 0.5 - 4.0),
            "D_skip": _w((lambda *s: torch.randn(*s, dtype=torch.bfloat16) * 0.1)(D)),
        }
    _B_in = _B_setup()

    return {
        "A_x": _A_in["x"],
        "A_wq": _A_in["wq"],
        "A_wk": _A_in["wk"],
        "A_wv": _A_in["wv"],
        "A_wo": _A_in["wo"],
        "B_u": _B_in["u"],
        "B_log_A_real": _B_in["log_A_real"],
        "B_A_imag": _B_in["A_imag"],
        "B_C_real": _B_in["C_real"],
        "B_C_imag": _B_in["C_imag"],
        "B_log_dt": _B_in["log_dt"],
        "B_D_skip": _B_in["D_skip"],
    }

def reference(A_x, A_wq, A_wk, A_wv, A_wo, B_u, B_log_A_real, B_A_imag, B_C_real, B_C_imag, B_log_dt, B_D_skip):
    def _A_ref(x, wq, wk, wv, wo):
        B, S, D = x.shape
        H = 16
        Dh = D // H
        q = (lambda x, *d: x.permute(*d))((x @ wq).reshape(B, S, H, Dh), 0, 2, 1, 3)
        k = x @ wk
        v = x @ wv
        scores = torch.einsum("bhqd,bkd->bhqk", q, k) * (Dh ** -0.5)
        p = _softmax(scores, axis=-1)
        o = torch.einsum("bhqk,bkd->bhqd", p, v)
        return (lambda x, *d: x.permute(*d))(o, 0, 2, 1, 3).reshape(B, S, H * Dh) @ wo

    def _B_ref(u, log_A_real, A_imag, C_real, C_imag, log_dt, D_skip):
        L = u.shape[1]
        dt = torch.exp(log_dt)
        A = -torch.exp(log_A_real) + 1j * A_imag
        C = (C_real + 1j * C_imag) * dt[:, None]
        pos = (lambda x, dt: x.to(dt))(torch.arange(L), log_dt.dtype)
        P = torch.exp((A * dt[:, None])[:, :, None] * pos[None, None, :])
        K = 2.0 * torch.einsum("dn,dnl->dl", C, P).real
        L2 = 2 * L
        u_t = (lambda x, *d: x.permute(*d))(u, 0, 2, 1)
        Kf = (lambda x, *a, **k: torch.fft.rfft(x.float(), *a, **k))(K, L2)
        Uf = (lambda x, *a, **k: torch.fft.rfft(x.float(), *a, **k))(u_t, L2)
        y = (lambda x, *a, **k: torch.fft.irfft(x, *a, **k).to(torch.bfloat16))(Uf * Kf[None, :, :], L2)[:, :, :L]
        y = (lambda x, *d: x.permute(*d))(y, 0, 2, 1)
        return y + u * D_skip[None, None, :]

    a = _A_ref(A_x, A_wq, A_wk, A_wv, A_wo)
    b = _B_ref(B_u, B_log_A_real, B_A_imag, B_C_real, B_C_imag, B_log_dt, B_D_skip)
    return torch.concatenate([a, b], axis=-1)
```

```cpp
#include <hip/hip_runtime.h>
#include <hip/hip_bf16.h>
#include <stdint.h>

typedef unsigned short u16;
__device__ __forceinline__ float bf2f(u16 v) { return __uint_as_float(((unsigned)v) << 16); }
__device__ __forceinline__ u16 f2bf(float f) {
    unsigned u = __float_as_uint(f);
    return (u16)((u + 0x7FFFu + ((u >> 16) & 1)) >> 16);
}
__device__ __forceinline__ float rbf(float f) { return bf2f(f2bf(f)); }

constexpr int S = 2048, D = 448, H = 16, DH = 28, N = 256;

__global__ void k_proj(const u16* x, const u16* wq, const u16* wk, const u16* wv, float* q, float* k, float* v) {
    int row = blockIdx.x;
    __shared__ float xs[D];
    for (int i = threadIdx.x; i < D; i += blockDim.x) xs[i] = bf2f(x[row * D + i]);
    __syncthreads();
    for (int c = threadIdx.x; c < D + 2 * DH; c += blockDim.x) {
        float acc = 0.f;
        if (c < D) { for (int i = 0; i < D; ++i) acc += xs[i] * bf2f(wq[i * D + c]); q[row * D + c] = rbf(acc); }
        else if (c < D + DH) { int cc = c - D; for (int i = 0; i < D; ++i) acc += xs[i] * bf2f(wk[i * DH + cc]); k[row * DH + cc] = rbf(acc); }
        else { int cc = c - D - DH; for (int i = 0; i < D; ++i) acc += xs[i] * bf2f(wv[i * DH + cc]); v[row * DH + cc] = rbf(acc); }
    }
}

__global__ void k_attn(const float* q, const float* k, const float* v, float* o) {
    int idx = blockIdx.x * blockDim.x + threadIdx.x;
    int row = idx / H, h = idx % H;
    float qr[DH], acc[DH];
#pragma unroll
    for (int i = 0; i < DH; ++i) { qr[i] = q[row * D + h * DH + i] * 0.1889822365f; acc[i] = 0.f; }
    float m = -1e30f, l = 0.f;
    for (int j = 0; j < S; ++j) {
        float s = 0.f;
#pragma unroll
        for (int i = 0; i < DH; ++i) s += qr[i] * k[j * DH + i];
        float mn = fmaxf(m, s);
        float a = __expf(m - mn), p = __expf(s - mn);
        l = l * a + p;
#pragma unroll
        for (int i = 0; i < DH; ++i) acc[i] = acc[i] * a + p * v[j * DH + i];
        m = mn;
    }
#pragma unroll
    for (int i = 0; i < DH; ++i) o[row * D + h * DH + i] = rbf(acc[i] / l);
}

__global__ void k_oproj(const float* o, const u16* wo, u16* out) {
    int row = blockIdx.x;
    __shared__ float xs[D];
    for (int i = threadIdx.x; i < D; i += blockDim.x) xs[i] = o[row * D + i];
    __syncthreads();
    for (int c = threadIdx.x; c < D; c += blockDim.x) {
        float acc = 0.f;
        for (int i = 0; i < D; ++i) acc += xs[i] * bf2f(wo[i * D + c]);
        out[row * 2 * D + c] = f2bf(acc);
    }
}

__global__ void k_gen(const u16* lar, const u16* aim, const u16* cr, const u16* ci, const u16* ldt, float* Kk) {
    int d = blockIdx.x;
    __shared__ float ar[N], ai[N], c_r[N], c_i[N];
    float dt = rbf(expf(bf2f(ldt[d])));
    for (int n = threadIdx.x; n < N; n += blockDim.x) {
        ar[n] = -rbf(expf(bf2f(lar[d * N + n]))) * dt;
        ai[n] = bf2f(aim[d * N + n]) * dt;
        c_r[n] = bf2f(cr[d * N + n]) * dt;
        c_i[n] = bf2f(ci[d * N + n]) * dt;
    }
    __syncthreads();
    for (int l = threadIdx.x; l < S; l += blockDim.x) {
        float p = rbf((float)l);
        float acc = 0.f;
        for (int n = 0; n < N; ++n) {
            float e = __expf(ar[n] * p);
            float r = ai[n] * p * 0.15915494309f;
            r = r - floorf(r);
            float sn = __builtin_amdgcn_sinf(r), cs = __builtin_amdgcn_cosf(r);
            acc += e * (c_r[n] * cs - c_i[n] * sn);
        }
        Kk[d * S + l] = 2.f * acc;
    }
}

__global__ void k_conv(const u16* u, const float* Kk, const u16* dsk, u16* out) {
    int d = blockIdx.x;
    __shared__ float us[S], ks[S];
    for (int t = threadIdx.x; t < S; t += blockDim.x) { us[t] = bf2f(u[t * D + d]); ks[t] = Kk[d * S + t]; }
    __syncthreads();
    float ds = bf2f(dsk[d]);
    for (int t = threadIdx.x; t < S; t += blockDim.x) {
        float acc = 0.f;
        for (int l = 0; l <= t; ++l) acc += ks[l] * us[t - l];
        out[t * 2 * D + D + d] = f2bf(rbf(acc) + rbf(us[t] * ds));
    }
}

extern "C" void kernel_launch(void* const* d_in, const int* in_sizes, int n_in,
                              void* d_out, int out_size, void* d_ws, size_t ws_size,
                              hipStream_t stream) {
    const u16* x = (const u16*)d_in[0];
    const u16* wq = (const u16*)d_in[1];
    const u16* wk = (const u16*)d_in[2];
    const u16* wv = (const u16*)d_in[3];
    const u16* wo = (const u16*)d_in[4];
    const u16* u = (const u16*)d_in[5];
    const u16* lar = (const u16*)d_in[6];
    const u16* aim = (const u16*)d_in[7];
    const u16* cr = (const u16*)d_in[8];
    const u16* ci = (const u16*)d_in[9];
    const u16* ldt = (const u16*)d_in[10];
    const u16* dsk = (const u16*)d_in[11];
    u16* out = (u16*)d_out;
    float* q = (float*)d_ws;
    float* k = q + S * D;
    float* v = k + S * DH;
    float* o = v + S * DH;
    float* Kk = o + S * D;
    k_proj<<<S, 256, 0, stream>>>(x, wq, wk, wv, q, k, v);
    k_attn<<<S * H / 64, 64, 0, stream>>>(q, k, v, o);
    k_oproj<<<S, 256, 0, stream>>>(o, wo, out);
    k_gen<<<D, 256, 0, stream>>>(lar, aim, cr, ci, ldt, Kk);
    k_conv<<<D, 256, 0, stream>>>(u, Kk, dsk, out);
}
```

```cpp
#include <hip/hip_runtime.h>
#include <hip/hip_bf16.h>
#include <stdint.h>

typedef unsigned short u16;
__device__ __forceinline__ float bf2f(u16 v) { return __uint_as_float(((unsigned)v) << 16); }
__device__ __forceinline__ u16 f2bf(float f) {
    unsigned u = __float_as_uint(f);
    return (u16)((u + 0x7FFFu + ((u >> 16) & 1)) >> 16);
}
__device__ __forceinline__ float rbf(float f) { return bf2f(f2bf(f)); }

constexpr int S = 2048, D = 448, H = 16, DH = 28, N = 256;

__global__ void k_proj(const u16* x, const u16* wq, const u16* wk, const u16* wv, float* q, float* k, float* v) {
    int row = blockIdx.x;
    __shared__ float xs[D];
    for (int i = threadIdx.x; i < D; i += blockDim.x) xs[i] = bf2f(x[row * D + i]);
    __syncthreads();
    for (int c = threadIdx.x; c < D + 2 * DH; c += blockDim.x) {
        float acc = 0.f;
        if (c < D) { for (int i = 0; i < D; ++i) acc += xs[i] * bf2f(wq[i * D + c]); q[row * D + c] = rbf(acc); }
        else if (c < D + DH) { int cc = c - D; for (int i = 0; i < D; ++i) acc += xs[i] * bf2f(wk[i * DH + cc]); k[row * DH + cc] = rbf(acc); }
        else { int cc = c - D - DH; for (int i = 0; i < D; ++i) acc += xs[i] * bf2f(wv[i * DH + cc]); v[row * DH + cc] = rbf(acc); }
    }
}

__global__ void k_attn(const float* q, const float* k, const float* v, float* o) {
    int idx = blockIdx.x * blockDim.x + threadIdx.x;
    int row = idx / H, h = idx % H;
    float qr[DH], acc[DH];
#pragma unroll
    for (int i = 0; i < DH; ++i) { qr[i] = q[row * D + h * DH + i] * 0.1889822365f; acc[i] = 0.f; }
    float m = -1e30f, l = 0.f;
    for (int j = 0; j < S; ++j) {
        float s = 0.f;
#pragma unroll
        for (int i = 0; i < DH; ++i) s += qr[i] * k[j * DH + i];
        float mn = fmaxf(m, s);
        float a = __expf(m - mn), p = __expf(s - mn);
        l = l * a + p;
#pragma unroll
        for (int i = 0; i < DH; ++i) acc[i] = acc[i] * a + p * v[j * DH + i];
        m = mn;
    }
#pragma unroll
    for (int i = 0; i < DH; ++i) o[row * D + h * DH + i] = rbf(acc[i] / l);
}

__global__ void k_oproj(const float* o, const u16* wo, u16* out) {
    int row = blockIdx.x;
    __shared__ float xs[D];
    for (int i = threadIdx.x; i < D; i += blockDim.x) xs[i] = o[row * D + i];
    __syncthreads();
    for (int c = threadIdx.x; c < D; c += blockDim.x) {
        float acc = 0.f;
        for (int i = 0; i < D; ++i) acc += xs[i] * bf2f(wo[i * D + c]);
        out[row * 2 * D + c] = f2bf(acc);
    }
}

__global__ void k_gen(const u16* lar, const u16* aim, const u16* cr, const u16* ci, const u16* ldt, float* Kk) {
    int d = blockIdx.x;
    __shared__ float ar[N], ai[N], c_r[N], c_i[N];
    float dt = rbf(expf(bf2f(ldt[d])));
    for (int n = threadIdx.x; n < N; n += blockDim.x) {
        ar[n] = -rbf(expf(bf2f(lar[d * N + n]))) * dt;
        ai[n] = bf2f(aim[d * N + n]) * dt;
        c_r[n] = bf2f(cr[d * N + n]) * dt;
        c_i[n] = bf2f(ci[d * N + n]) * dt;
    }
    __syncthreads();
    for (int l = threadIdx.x; l < S; l += blockDim.x) {
        float p = rbf((float)l);
        float acc = 0.f;
        for (int n = 0; n < N; ++n) {
            float e = __expf(ar[n] * p);
            float r = ai[n] * p * 0.15915494309f;
            r = r - floorf(r);
            float sn = __builtin_amdgcn_sinf(r), cs = __builtin_amdgcn_cosf(r);
            acc += e * (c_r[n] * cs - c_i[n] * sn);
        }
        Kk[d * S + l] = 2.f * acc;
    }
}

__global__ void k_conv(const u16* u, const float* Kk, const u16* dsk, u16* out) {
    int d = blockIdx.x;
    __shared__ float us[S], ks[S];
    for (int t = threadIdx.x; t < S; t += blockDim.x) { us[t] = bf2f(u[t * D + d]); ks[t] = Kk[d * S + t]; }
    __syncthreads();
    float ds = bf2f(dsk[d]);
    for (int t = threadIdx.x; t < S; t += blockDim.x) {
        float acc = 0.f;
        for (int l = 0; l <= t; ++l) acc += ks[l] * us[t - l];
        out[t * 2 * D + D + d] = f2bf(rbf(acc) + rbf(us[t] * ds));
    }
}

#define STAT 0

typedef unsigned u32; typedef unsigned long long u64;
__device__ __forceinline__ u64 rt() { u64 t; asm volatile("s_memrealtime %0\n s_waitcnt lgkmcnt(0)" : "=s"(t) :: "memory"); return t; }
__device__ __forceinline__ u64 ct() { u64 t; asm volatile("s_memtime %0\n s_waitcnt lgkmcnt(0)" : "=s"(t) :: "memory"); return t; }
__device__ __forceinline__ void glds16(const void* g, u32 lds) {
    u32 sv; asm volatile("s_mov_b32 %0, m0\n s_mov_b32 m0, %2\n s_nop 0\n global_load_lds_dwordx4 %1, off\n s_mov_b32 m0, %0" : "=&s"(sv) : "v"(g), "s"(lds) : "memory"); }
#define VM0() asm volatile("s_waitcnt vmcnt(0)" ::: "memory")
__global__ void __launch_bounds__(512, 2) k_probe(const char* x, const char* wq, const char* wk, u64* st) {
    __shared__ __attribute__((aligned(16))) char lds[158720];
    const int g = blockIdx.x, tid = threadIdx.x, lane = tid & 63, w = __builtin_amdgcn_readfirstlane(tid >> 6);
    const int xcd = g & 7, j = g >> 3, qt = xcd * 8 + (j >> 2), hg = j & 3;
    u64 t0 = rt(), c0 = ct();
    const u32 l0 = (u32)(uintptr_t)lds;
    if (w < 4) {
        for (int i = 0; i < 7; ++i) { int pc = w * 7 + i; glds16(x + (size_t)qt * 28672 + pc * 1024 + lane * 16, l0 + pc * 1024); }
        for (int i = 0; i < 6; ++i) { int pc = w * 6 + i; glds16(wk + pc * 1024 + lane * 16, l0 + 28672 + pc * 1024); }
    } else {
        for (int i = 0; i < 25; ++i) { int pc = (w - 4) * 25 + i; if (pc < 98) { int ch = pc * 64 + lane; int row = ch / 14, c = ch - row * 14;
            glds16(wq + row * 896 + hg * 224 + c * 16, l0 + 57344 + pc * 1024); } }
    }
    VM0();
    u64 t1 = rt(), c1 = ct();
    if (lane == 0) { st[(g * 8 + w) * 4] = t0; st[(g * 8 + w) * 4 + 1] = t1; st[(g * 8 + w) * 4 + 2] = c1 - c0; st[(g * 8 + w) * 4 + 3] = lds[tid]; }
}
__global__ void k_amp(const u64* st) {
    __shared__ u64 a[256]; __shared__ u64 res;
    int g = threadIdx.x;
    u64 tmin = ~0ull;
    for (int i = 0; i < 2048; ++i) tmin = min(tmin, st[i * 4]);
    u64 v = 0;
    if (STAT == 1) v = st[g * 32] - tmin;
    if (STAT == 2 || STAT == 3) for (int w = 0; w < 4; ++w) v = max(v, st[(g * 8 + w) * 4 + 1] - tmin);
    if (STAT == 4 || STAT == 5) for (int w = 4; w < 8; ++w) v = max(v, st[(g * 8 + w) * 4 + 1] - tmin);
    if (STAT == 6) v = st[(g * 8 + 4) * 4 + 2] * 10 / (st[(g * 8 + 4) * 4 + 1] - st[(g * 8 + 4) * 4]);
    a[g] = v;
    __syncthreads();
    int rank = 0;
    for (int i = 0; i < 256; ++i) rank += (a[i] < v) || (a[i] == v && i < g);
    int want = (STAT == 1 || STAT == 3 || STAT == 5) ? 255 : 128;
    if (rank == want) res = v;
    __syncthreads();
    if (threadIdx.x < 64) { u64 d = min(res, (u64)2000) * 100; u64 s = rt(); while (rt() - s < d) __builtin_amdgcn_s_sleep(4); }
}

extern "C" void kernel_launch(void* const* d_in, const int* in_sizes, int n_in,
                              void* d_out, int out_size, void* d_ws, size_t ws_size,
                              hipStream_t stream) {
    const u16* x = (const u16*)d_in[0];
    const u16* wq = (const u16*)d_in[1];
    const u16* wk = (const u16*)d_in[2];
    const u16* wv = (const u16*)d_in[3];
    const u16* wo = (const u16*)d_in[4];
    const u16* u = (const u16*)d_in[5];
    const u16* lar = (const u16*)d_in[6];
    const u16* aim = (const u16*)d_in[7];
    const u16* cr = (const u16*)d_in[8];
    const u16* ci = (const u16*)d_in[9];
    const u16* ldt = (const u16*)d_in[10];
    const u16* dsk = (const u16*)d_in[11];
    u16* out = (u16*)d_out;
    float* q = (float*)d_ws;
    float* k = q + S * D;
    float* v = k + S * DH;
    float* o = v + S * DH;
    float* Kk = o + S * D;
    u64* st = (u64*)((char*)d_ws + (64 << 20));
    k_probe<<<256, 512, 0, stream>>>((const char*)x, (const char*)wq, (const char*)wk, st);
    k_amp<<<1, 256, 0, stream>>>(st);
    k_proj<<<S, 256, 0, stream>>>(x, wq, wk, wv, q, k, v);
    k_attn<<<S * H / 64, 64, 0, stream>>>(q, k, v, o);
    k_oproj<<<S, 256, 0, stream>>>(o, wo, out);
    k_gen<<<D, 256, 0, stream>>>(lar, aim, cr, ci, ldt, Kk);
    k_conv<<<D, 256, 0, stream>>>(u, Kk, dsk, out);
}
```

```cpp
#include <hip/hip_runtime.h>
#include <stdint.h>

typedef unsigned short u16;
typedef unsigned u32;
typedef unsigned long long u64;
typedef u32 u32x2 __attribute__((ext_vector_type(2)));
typedef u32 u32x4 __attribute__((ext_vector_type(4)));
typedef float f32x2 __attribute__((ext_vector_type(2)));
typedef float f32x4 __attribute__((ext_vector_type(4)));
typedef float f32x8 __attribute__((ext_vector_type(8)));
typedef float f32x16 __attribute__((ext_vector_type(16)));
typedef __bf16 bf16x2 __attribute__((ext_vector_type(2)));
typedef __bf16 bf16x8 __attribute__((ext_vector_type(8)));
typedef __bf16 bf16x16 __attribute__((ext_vector_type(16)));
typedef short s16x4 __attribute__((ext_vector_type(4)));
#define LAS __attribute__((address_space(3)))
#define CAS __attribute__((address_space(4)))

constexpr u32 W_F1 = 4096;
constexpr u32 W_F2 = 16384;
constexpr u32 W_KV = 65536;
constexpr u32 W_QX = 1u << 20;
constexpr u32 W_UX = 5u << 20;
constexpr u32 W_YX = 9u << 20;
constexpr u32 W_OX = 11u << 20;
constexpr u32 W_END = 13u << 20;

constexpr u32 L_XS = 0;
constexpr u32 L_WS = 57344;
constexpr u32 L_SCR = 114688;
constexpr u32 L_US = 149504;
constexpr u32 L_KC = 159744;
constexpr u32 L_QS = 0;
constexpr u32 L_WO = 0;
constexpr u32 L_RS = 114688;
constexpr u32 L_OS = 131072;
constexpr u32 L_END = 163840;

constexpr int SPIN_CAP = 1 << 20;
constexpr float QSCALE = 0.18898223650461362f * 1.4426950408889634f;

#define BAR() asm volatile("s_waitcnt lgkmcnt(0)\n\ts_barrier" ::: "memory")
#define VMWAIT(n) asm volatile("s_waitcnt vmcnt(" #n ")" ::: "memory")
#define FENCE() asm volatile("" ::: "memory")
#define MFMA32(a, b, c) __builtin_amdgcn_mfma_f32_32x32x16_bf16(__builtin_bit_cast(bf16x8, a), __builtin_bit_cast(bf16x8, b), c, 0, 0, 0)
#define MFMA16(a, b, c) __builtin_amdgcn_mfma_f32_16x16x32_bf16(__builtin_bit_cast(bf16x8, a), __builtin_bit_cast(bf16x8, b), c, 0, 0, 0)

#define CSTAMP(i) do { } while (0)
#define STAMP(i) do { } while (0)

__device__ __forceinline__ float bf2f(u32 v) { return __uint_as_float(v << 16); }
__device__ __forceinline__ u32 pk2(float lo, float hi) {
    f32x2 v = {lo, hi};
    return __builtin_bit_cast(u32, __builtin_convertvector(v, bf16x2));
}
__device__ __forceinline__ float rbf(float f) { return bf2f(pk2(f, f) & 0xffffu); }
template <class T> __device__ __forceinline__ T lds_ld(u32 a) { return *(const LAS T*)(uintptr_t)a; }
template <class T> __device__ __forceinline__ void lds_st(u32 a, T v) { *(LAS T*)(uintptr_t)a = v; }
__device__ __forceinline__ u32x2 lds_tr(u32 a) {
    return __builtin_bit_cast(u32x2, __builtin_amdgcn_ds_read_tr16_b64_v4i16((LAS s16x4*)(uintptr_t)a));
}
__device__ __forceinline__ u32x4 cat(u32x2 a, u32x2 b) { return (u32x4){a.x, a.y, b.x, b.y}; }
__device__ __forceinline__ void dma(const char* g, u32 l) {
    asm volatile("s_mov_b32 m0, %1\n\ts_nop 0\n\tglobal_load_lds_dwordx4 %0, off" :: "v"(g), "s"(l) : "memory");
}
__device__ __forceinline__ void dma_dev(const char* g, u32 l) {
    asm volatile("s_mov_b32 m0, %1\n\ts_nop 0\n\tglobal_load_lds_dwordx4 %0, off sc1" :: "v"(g), "s"(l) : "memory");
}
typedef __amdgpu_buffer_rsrc_t rsrc_t;
__device__ __forceinline__ void st128(rsrc_t r, u32 off, u32x4 v) { __builtin_amdgcn_raw_buffer_store_b128(v, r, off, 0, 16); }
__device__ __forceinline__ void st32(rsrc_t r, u32 off, u32 v) { __builtin_amdgcn_raw_buffer_store_b32(v, r, off, 0, 16); }
__device__ __forceinline__ u32x4 ld128(rsrc_t r, u32 off) { return __builtin_amdgcn_raw_buffer_load_b128(r, off, 0, 16); }
__device__ __forceinline__ u32 ld32(rsrc_t r, u32 off) { return __builtin_amdgcn_raw_buffer_load_b32(r, off, 0, 16); }

__device__ __forceinline__ f32x2 cmul(f32x2 z, f32x2 w) {
    f32x2 o;
    asm("v_pk_mul_f32 %0, %1, %2 op_sel_hi:[1,0]\n\tv_pk_fma_f32 %0, %1, %2, %0 op_sel:[1,1,0] op_sel_hi:[0,1,1] neg_lo:[0,1,0]" : "=&v"(o) : "v"(z), "v"(w));
    return o;
}
__device__ __forceinline__ f32x2 cmulc(f32x2 z, f32x2 w) {
    f32x2 o;
    asm("v_pk_mul_f32 %0, %1, %2 op_sel_hi:[1,0]\n\tv_pk_fma_f32 %0, %1, %2, %0 op_sel:[1,1,0] op_sel_hi:[0,1,1] neg_hi:[0,1,0]" : "=&v"(o) : "v"(z), "v"(w));
    return o;
}
__device__ __forceinline__ f32x2 cexp_at(float a2, float rev, float p) {
    float e = __builtin_amdgcn_exp2f(a2 * p), r = __builtin_amdgcn_fractf(rev * p);
    return (f32x2){e * __builtin_amdgcn_cosf(r), e * __builtin_amdgcn_sinf(r)};
}

__global__ void __launch_bounds__(768, 3) fused_kernel(
    const char* __restrict__ x, const char* __restrict__ wq, const char* __restrict__ wk, const char* __restrict__ wv,
    const char* __restrict__ wo, const char* __restrict__ u, const u16* __restrict__ lar, const u16* __restrict__ aim,
    const u16* __restrict__ cre, const u16* __restrict__ cim, const char* __restrict__ ldt, const char* __restrict__ dsk,
    char* __restrict__ out, char* ws) {
    __shared__ __attribute__((aligned(1024))) char lds[L_END];
    asm volatile("" :: "s"((u32)(uintptr_t)lds));
    const u32 tid = threadIdx.x, lane = tid & 63, w = __builtin_amdgcn_readfirstlane(tid >> 6);
    const u32 g = blockIdx.x, xcd = g & 7, j = g >> 3;
    const bool kvwg = j < 4;
    const u32 jj = j - 4;
    const u32 rt = 4 * xcd + (kvwg ? j : jj / 7), cs = kvwg ? 7 : jj % 7;
    const u32 cp = 28 * xcd + jj;
    const u32 qt = 8 * xcd + (j >> 2), hg = j & 3;
    const u32 r32 = lane & 31, hh = lane >> 5, c16 = lane & 15, g4 = lane >> 4;
    STAMP(0); CSTAMP(14);
    u32 tag = *(const CAS u32*)ws;
    const rsrc_t rw = __builtin_amdgcn_make_buffer_rsrc(ws, 0, W_END, 0x00020000);

    if (w >= 8) {
        const u32 lw = w - 8, rin = lane >> 3, pos = lane & 7;
        const char* xs = x + (64 * rt + 8 * lw + rin) * 896 + ((pos ^ ((4 * lw + g4) & 7)) << 4);
        const u32 chunk = pos ^ ((g4 & 1) << 2), cc = chunk & 3;
        const char* wsrc = kvwg ? ((chunk & 4) ? wv : wk) + (8 * lw + rin) * 56 + (cc < 3 ? 16 * cc : 40)
                                : wq + (8 * lw + rin) * 896 + 128 * cs + 16 * chunk;
        const u32 wp = kvwg ? 56 : 896;
        u32 a = (cs + 2 * (rt & 3) + xcd) % 7;
#pragma unroll 1
        for (int i = 0; i < 7; ++i) {
            const u32 l = a * 8192 + lw * 1024;
            dma(xs + a * 128, L_XS + l);
            dma(wsrc + a * 64 * wp, L_WS + l);
            dma(xs + a * 128 + 32 * 896, L_XS + l + 4096);
            dma(wsrc + (a * 64 + 32) * wp, L_WS + l + 4096);
            a = a == 6 ? 0 : a + 1;
        }
        asm volatile("" : "+s"(tag));
        tag = tag * 1664525u + 1013904223u;
        STAMP(1);
        VMWAIT(0);
    } else {
        if (!kvwg) {
            const char* us = u + (64 * rt + 2 * r32) * 896 + 128 * cs + 16 * w + 8 * hh;
            const u32x2 ua = *(const u32x2*)us, ub = *(const u32x2*)(us + 896);
            const u32 ch = w >> 2, pi = (2 * cp + ch) * 256 + 64 * (w & 3) + lane;
            const u32 vlar = lar[pi], vaim = aim[pi], vcr = cre[pi], vci = cim[pi];
            const u32 ldt2 = *(const CAS u32*)(ldt + 4 * cp);
            if (tid < 128) lds_st<u32x4>(L_US + (tid >> 4) * 1280 + (tid & 15) * 16, (u32x4){0, 0, 0, 0});
            if (tid < 2) lds_st<u32x4>(L_KC + tid * 1552, (u32x4){0, 0, 0, 0});
            asm volatile("" : "+s"(tag));
            tag = tag * 1664525u + 1013904223u;
            const u32 uo = W_UX + (32 * cs + 4 * w + 2 * hh) * 16384 + (32 * rt + r32) * 16;
            st128(rw, uo, (u32x4){__builtin_amdgcn_perm(ub.x, ua.x, 0x05040100), tag, __builtin_amdgcn_perm(ub.x, ua.x, 0x07060302), tag});
            st128(rw, uo + 16384, (u32x4){__builtin_amdgcn_perm(ub.y, ua.y, 0x05040100), tag, __builtin_amdgcn_perm(ub.y, ua.y, 0x07060302), tag});
            STAMP(1);
            const float dt = rbf(expf(bf2f(ch ? ldt2 >> 16 : ldt2 & 0xffffu)));
            const float a2 = -rbf(expf(bf2f(vlar))) * dt * 1.4426950408889634f;
            const float rev = bf2f(vaim) * dt * 0.15915494309189535f;
            const f32x2 w1 = cexp_at(a2, rev, 1.f), w8 = cexp_at(a2, rev, 8.f), w128 = cexp_at(a2, rev, 128.f);
            const u32 sw = L_SCR + w * 4352 + lane * 4, sr = L_SCR + w * 4352 + c16 * 272 + g4 * 16;
#define PUT(row, z) lds_st<u32>(sw + (row) * 272, pk2((z).x, (z).y))
            u32x4 bf[4];
            {
                f32x2 ze = cmul(w8, w8), zo = w8;
                const f32x2 w16 = ze;
                lds_st<u32>(sw, 0x3f80u);
                PUT(1, zo);
#pragma unroll
                for (int i = 2; i < 16; i += 2) {
                    zo = cmul(zo, w16);
                    PUT(i, ze); PUT(i + 1, zo);
                    if (i < 14) ze = cmul(ze, w16);
                }
#pragma unroll
                for (int k = 0; k < 4; ++k) bf[k] = lds_ld<u32x4>(sr + 64 * k);
            }
            f32x4 acc[3];
#define TILE(t) do { acc[t] = (f32x4){0, 0, 0, 0}; _Pragma("unroll") for (int k = 0; k < 4; ++k) acc[t] = MFMA16(lds_ld<u32x4>(sr + 64 * k), bf[k], acc[t]); } while (0)
            f32x2 e[4];
            {
                f32x2 z = {2.f * (bf2f(vcr) * dt), -2.f * (bf2f(vci) * dt)};
#pragma unroll
                for (int i = 0; i < 8; ++i) {
                    const f32x2 y = cmulc(z, w128);
                    PUT(i, z); PUT(8 + i, y);
                    if (!(i & 1)) e[i >> 1] = y;
                    if (i < 7) z = cmulc(z, w1);
                }
            }
            TILE(0);
#pragma unroll
            for (int s = 0; s < 2; ++s)
#pragma unroll
                for (int i = 0; i < 4; ++i) { e[i] = cmulc(e[i], w128); PUT(4 * s + i, e[i]); }
#pragma unroll
            for (int s = 0; s < 4; ++s) { e[0] = cmulc(e[0], w128); e[2] = cmulc(e[2], w128); PUT(8 + 2 * s, e[0]); PUT(9 + 2 * s, e[2]); }
            TILE(1);
#pragma unroll
            for (int i = 0; i < 9; ++i) { e[0] = cmulc(e[0], w128); PUT(i, e[0]); }
            TILE(2);
#pragma unroll
            for (int t = 0; t < 3; ++t) lds_st<f32x4>(L_SCR + w * 4352 + t * 1024 + lane * 16, acc[t]);
        } else {
            asm volatile("" : "+s"(tag));
            tag = tag * 1664525u + 1013904223u;
        }
    }
    STAMP(2);
    BAR();
    STAMP(3);

    if (w < 4) {
        const u32 mt = w >> 1, nt = w & 1;
        const bool isv = kvwg && nt;
        const u32 xr = 32 * mt + (isv ? ((r32 & 19) | ((r32 & 4) << 1) | ((r32 & 8) >> 1)) : r32);
        const u32 cb = g4 & 1, q = (lane >> 2) & 3, p = lane & 3, ps = (kvwg && cb) ? p ^ (p >> 1) : p;
        u32 xa = L_XS + xr * 128 + ((hh ^ ((xr >> 1) & 7)) << 4);
        u32 wa = L_WS + (8 * hh + q) * 128 + ((64 * nt + 32 * cb + 8 * ps) ^ ((q >> 1) << 6));
        f32x16 acc = {0};
#pragma unroll 1
        for (int a = 0; a < 7; ++a) {
            u32x4 xf[4], wf[4];
#pragma unroll
            for (int b = 0; b < 4; ++b) {
                xf[b] = lds_ld<u32x4>(xa ^ (b << 5));
                wf[b] = cat(lds_tr(wa + b * 2048), lds_tr(wa + b * 2048 + 512));
            }
            if (isv) {
#pragma unroll
                for (int b = 0; b < 4; ++b) acc = MFMA32(xf[b], wf[b], acc);
            } else {
#pragma unroll
                for (int b = 0; b < 4; ++b) acc = MFMA32(wf[b], xf[b], acc);
            }
            xa += 8192; wa += 8192;
            if (a == 1) BAR();
        }
        STAMP(4);
        BAR();
        if (!kvwg) {
            const u32 qo = W_QX + (((2 * rt + mt) * 112 + 16 * cs + 8 * nt) * 32 + lane) * 16;
#pragma unroll
            for (int r = 0; r < 4; ++r)
                st128(rw, qo + r * 1024, (u32x4){pk2(acc[4 * r] * QSCALE, acc[4 * r + 1] * QSCALE), tag, pk2(acc[4 * r + 2] * QSCALE, acc[4 * r + 3] * QSCALE), tag});
        } else {
            u32x4 f0 = {pk2(acc[0], acc[1]), pk2(acc[2], acc[3]), pk2(acc[4], acc[5]), pk2(acc[6], acc[7])};
            u32x4 f1 = {pk2(acc[8], acc[9]), pk2(acc[10], acc[11]), pk2(acc[12], acc[13]), pk2(acc[14], acc[15])};
            u32 slot = lane;
            if (nt) {
                const u32 s = r32 / 14, rem = r32 - 14 * s, h2 = rem / 7, i = rem - 7 * h2;
                slot = 16 * s + 8 * (i >> 2) + 4 * h2 + (i & 3);
                if (r32 >= 28) {
                    slot = 11 + 4 * (r32 & 1) + 8 * (r32 & 2);
                    f0 = f1 = (u32x4){0x3f803f80u, 0x3f803f80u, 0x3f803f80u, 0x3f803f80u};
                }
                slot += 32 * hh;
            } else if (hh) {
                f1.z = 0; f1.w = 0;
            }
            const u32 ko = W_KV + (2 * rt + mt) * 4096 + nt * 2048 + slot * 16;
            st128(rw, ko, f0);
            st128(rw, ko + 1024, f1);
            VMWAIT(0);
            BAR();
        }
        const u32 oo = W_OX + (qt * 32 + 8 * hg + 2 * w) * 1024 + lane * 16;
        st128(rw, oo, (u32x4){~0u, ~0u, ~0u, ~0u});
        st128(rw, oo + 1024, (u32x4){~0u, ~0u, ~0u, ~0u});
        STAMP(5);
        const u32 qi = W_QX + ((qt * 112 + 7 * (4 * hg + w)) * 32 + lane) * 16;
        u32x4 q0, q1, q2, q3;
        for (int spin = 0; spin < SPIN_CAP; ++spin) {
            FENCE();
            q0 = ld128(rw, qi); q1 = ld128(rw, qi + 1024); q2 = ld128(rw, qi + 2048); q3 = ld128(rw, qi + 3072 - 512 * hh);
            const bool ok = q0.y == tag && q0.w == tag && q1.y == tag && q1.w == tag && q2.y == tag && q2.w == tag && q3.y == tag && q3.w == tag;
            if (__all(ok)) break;
        }
        lds_st<u32x4>(L_QS + w * 2048 + lane * 16, (u32x4){q0.x, q0.z, q1.x, q1.z});
        lds_st<u32x4>(L_QS + w * 2048 + 1024 + lane * 16, (u32x4){q2.x, q2.z, hh ? 0u : q3.x, hh ? 0u : q3.z});
    } else {
        if (!kvwg) {
            const u32 t2 = tid - 256;
            const u32 ui = W_UX + cp * 16384 + t2 * 16;
            u32x4 u0 = ld128(rw, ui), u1 = ld128(rw, ui + 8192);
            if (w < 10) {
                const u32 c = (w - 4) / 3, t = (w - 4) - 3 * c;
                const u32 pa = L_SCR + c * 4 * 4352 + t * 1024 + lane * 16;
                f32x4 s = (lds_ld<f32x4>(pa) + lds_ld<f32x4>(pa + 4352)) + (lds_ld<f32x4>(pa + 2 * 4352) + lds_ld<f32x4>(pa + 3 * 4352));
                const u32 dsk2 = *(const CAS u32*)(dsk + 4 * cp);
                if (t == 0 && lane == 0) s.x += bf2f(c ? dsk2 >> 16 : dsk2 & 0xffffu);
                u32 base, s1 = 1, s2 = 2;
                if (t == 0) base = 128 * (g4 >> 1) + 8 * c16 + 4 * (g4 & 1);
                else if (t == 1) { base = 256 + 64 * g4 + (g4 < 2 ? 4 : 2) * c16; s2 = g4 < 2 ? 2 : 32; }
                else { base = 512 + 64 * g4 + c16; s1 = 16; s2 = 32; }
                const u32 ka = L_KC + c * 1552 + 16 + 2 * base, lo = pk2(s.x, s.y), hi = pk2(s.z, s.w);
                lds_st<u16>(ka, lo); lds_st<u16>(ka + 2 * s1, lo >> 16);
                lds_st<u16>(ka + 2 * s2, hi); lds_st<u16>(ka + 2 * (s1 + s2), hi >> 16);
            }
            BAR();
            {
                const u32 c = t2 >> 8, m = t2 & 255;
                const int o = max(0, 131 - 8 * (int)m);
                const float f0 = (float)(8 * (int)m - 3 + o);
                const u32 kb = L_KC + c * 1552 + 16 - 2 * (17024 + o);
                u32 kv[12];
#pragma unroll
                for (int e = 0; e < 12; e += 2) {
                    const u32 b = pk2(f0 + e, f0 + (e + 1));
                    kv[e] = lds_ld<u16>(kb + 2 * (b & 0xffffu));
                    if (e < 10) kv[e + 1] = lds_ld<u16>(kb + 2 * (b >> 16));
                }
                const u32 ta = L_SCR + c * 16640 + (m + 4) * 8;
#pragma unroll
                for (int i = 0; i < 8; ++i) lds_st<u32x2>(ta + i * 2080, (u32x2){kv[i + 3] | (kv[i + 2] << 16), kv[i + 1] | (kv[i] << 16)});
                if (m < 32) lds_st<u32x2>(L_SCR + c * 16640 + (m >> 2) * 2080 + (m & 3) * 8, (u32x2){0, 0});
            }
            for (int spin = 0; spin < SPIN_CAP; ++spin) {
                if (__all(u0.y == tag && u0.w == tag && u1.y == tag && u1.w == tag)) break;
                FENCE();
                u0 = ld128(rw, ui); u1 = ld128(rw, ui + 8192);
            }
            const u32 ua = L_US + ((t2 >> 2) & 3) * 1280 + (16 + (t2 >> 4)) * 16 + (t2 & 3) * 4;
            lds_st<u32>(ua, u0.x); lds_st<u32>(ua + 5120, u0.z);
            lds_st<u32>(ua + 512, u1.x); lds_st<u32>(ua + 5120 + 512, u1.z);
            STAMP(4);
            BAR();
        } else {
            BAR();
            BAR();
            BAR();
            if (tid == 512) st32(rw, W_F1 + rt * 128, tag);
        }
        if (w < 8) {
            if (!kvwg) {
                const u32 c = (w >> 1) & 1, al = w & 1;
                const u32 x0 = 32 + 16 * al + c16 - 8 * g4, x1 = x0 - 4;
                u32 a0 = L_SCR + c * 16640 + (x0 & 7) * 2080 + (x0 >> 3) * 8;
                u32 a1 = L_SCR + c * 16640 + (x1 & 7) * 2080 + (x1 >> 3) * 8;
                u32 ba = L_US + c * 5120 + g4 * 1280 + (16 + c16) * 16;
                f32x4 y[4] = {{0, 0, 0, 0}, {0, 0, 0, 0}, {0, 0, 0, 0}, {0, 0, 0, 0}};
                u32x4 A[4], B[4], An[4], Bn[4];
#define CLOAD(A_, B_, r) _Pragma("unroll") for (int s = 0; s < 4; ++s) { \
                    A_[s] = cat(lds_ld<u32x2>(a0 + 32 * (r) + 512 * s), lds_ld<u32x2>(a1 + 32 * (r) + 512 * s)); B_[s] = lds_ld<u32x4>(ba + 16 - 16 * (r) + 256 * s); }
#define CMMA(A_, B_) _Pragma("unroll") for (int s = 0; s < 4; ++s) _Pragma("unroll") for (int i = s; i < 4; ++i) y[i] = MFMA16(A_[s], B_[i - s], y[i]);
                ba -= 16;
                CLOAD(A, B, 0);
#pragma unroll 1
                for (int r = 0; r < 8; ++r) {
                    CLOAD(An, Bn, 1);
                    CMMA(A, B);
                    a0 += 64; a1 += 64; ba -= 32;
                    CLOAD(A, B, 0);
                    CMMA(An, Bn);
                }
                const u32 d = 2 * cp + c;
                const u32 yo = W_YX + (((d >> 6) * 32 + 2 * c16 + al) * 64 + (d & 63)) * 128 + g4 * 16;
                st128(rw, yo, (u32x4){pk2(y[0].x, y[0].y), pk2(y[0].z, y[0].w), pk2(y[1].x, y[1].y), pk2(y[1].z, y[1].w)});
                st128(rw, yo + 64, (u32x4){pk2(y[2].x, y[2].y), pk2(y[2].z, y[2].w), pk2(y[3].x, y[3].y), pk2(y[3].z, y[3].w)});
            }
        }
    }

    if (w >= 8) {
        const u32 lw = w - 8;
        for (int spin = 0; spin < SPIN_CAP; ++spin) {
            FENCE();
            if (__all(ld32(rw, W_F1 + r32 * 128) == tag)) break;
        }
        STAMP(5);
        const char* kvs = ws + W_KV + lw * 4096 + lane * 16;
        const u32 colb = (c16 ^ (g4 << 2)) << 4;
        const char* wos = wo + (16 * lw + g4) * 896 + 256 * hg + (hg == 3 ? colb & 127 : colb);
#define KVSTAGE(n, slot) _Pragma("unroll") for (int i = 0; i < 4; ++i) dma_dev(kvs + (n) * 16384 + i * 1024, (slot) * 16384 + lw * 4096 + i * 1024);
        KVSTAGE(0, 4); KVSTAGE(1, 5); KVSTAGE(2, 6);
        VMWAIT(8);
        STAMP(6);
        BAR();
        KVSTAGE(3, 7);
        const u32 ycp = 32 * cs + r32, yx = ycp / 28;
        const u32 fw = hh ? xcd + 8 * ((j & ~3u) + (lane & 3)) : (kvwg ? g : yx + 8 * (ycp - 28 * yx + 4));
        u32 f2 = 0, slot = 8;
#pragma unroll 1
        for (u32 it = 0; it < 16; ++it) {
            VMWAIT(8);
            asm volatile("s_barrier" ::: "memory");
            if (it < 12) { KVSTAGE(it + 4, slot); slot = slot == 9 ? 0 : slot + 1; }
            if (it >= 8 && it < 15) {
#pragma unroll
                for (int i = 0; i < 4; ++i) dma(wos + ((it - 8) * 64 + 4 * i) * 896, L_WO + (it - 8) * 16384 + lw * 4096 + i * 1024);
            }
            if (it == 4 && tid == 512) st32(rw, W_F2 + g * 128, tag);
            if (it == 12) asm volatile("global_load_dword %0, %1, off sc1" : "+v"(f2) : "v"(ws + W_F2 + fw * 128) : "memory");
        }
        asm volatile("s_waitcnt vmcnt(0)" : "+v"(f2) :: "memory");
        for (int spin = 0; spin < SPIN_CAP; ++spin) {
            if (__all(f2 == tag)) break;
            FENCE();
            f2 = ld32(rw, W_F2 + fw * 128);
        }
        STAMP(7);
        BAR();
        if (!kvwg) {
            const u32 pos = 2 * lw + hh, yi = W_YX + ((cs * 32 + rt) * 64 + 2 * r32) * 128 + pos * 16;
            const u32x4 ya = ld128(rw, yi), yb = ld128(rw, yi + 128);
            char* yo = out + (16 * rt + 4 * (pos & 3) + 1024 * (pos >> 2)) * 1792 + 896 + 128 * cs + 4 * r32;
#pragma unroll
            for (int r = 0; r < 8; ++r)
                *(u32*)(yo + ((r & 3) + 512 * (r >> 2)) * 1792) = __builtin_amdgcn_perm(yb[r >> 1], ya[r >> 1], (r & 1) ? 0x07060302 : 0x05040100);
        } else if (g == 0 && lw == 0) {
            for (int spin = 0; spin < SPIN_CAP; ++spin) {
                FENCE();
                bool ok = true;
#pragma unroll
                for (int i = 0; i < 4; ++i) ok &= ld32(rw, W_F2 + (lane + 64 * i) * 128) == tag;
                if (__all(ok)) break;
            }
            if (lane == 0) st32(rw, 0, tag);
        }
        STAMP(8);
        BAR();
        BAR();
    } else {
        const u32 hd = w & 3, kh = w >> 2;
        STAMP(6);
        f32x16 o;
        u32x4 qf0, qf1;
        {
            const u32 lb = lane * 16 + kh * 8192, qa = L_QS + hd * 2048 + lane * 16;
#define E4(d, s) "v_exp_f32 v[" #d "+0], v[" #s "+0]\n\tv_exp_f32 v[" #d "+1], v[" #s "+1]\n\tv_exp_f32 v[" #d "+2], v[" #s "+2]\n\tv_exp_f32 v[" #d "+3], v[" #s "+3]\n\t"
#define HALF(S, SN, KO, V1B, V1O, V0B, V0O) \
            "s_waitcnt lgkmcnt(3)\n\tv_mfma_f32_32x32x16_bf16 v[" #SN ":" #SN "+15], v[112:115], v[104:107], 0\n\t" \
            "ds_read_b128 v[112:115], v129 offset:" #KO "\n\t" E4(72, S) \
            "s_waitcnt lgkmcnt(3)\n\tv_mfma_f32_32x32x16_bf16 v[" #SN ":" #SN "+15], v[116:119], v[108:111], v[" #SN ":" #SN "+15]\n\t" \
            "ds_read_b128 v[116:119], v129 offset:" #KO "+1024\n\t" E4(76, S+4) \
            "s_waitcnt lgkmcnt(3)\n\tv_smfmac_f32_32x32x32_bf16 v[88:103], v[124:127], v[80:87], v128\n\t" \
            "ds_read_b128 v[124:127], " #V1B " offset:" #V1O "\n\t" E4(80, S+8) \
            "s_waitcnt lgkmcnt(3)\n\tv_smfmac_f32_32x32x32_bf16 v[88:103], v[120:123], v[72:79], v128\n\t" \
            "ds_read_b128 v[120:123], " #V0B " offset:" #V0O "\n\t" E4(84, S+12)
            u32 sit, snx;
            asm volatile(
                "v_add_u32 v129, 0x10000, %[lb]\n\t"
                "v_mov_b32 v128, 0xdddd\n\t"
                ".irp i,80,81,82,83,84,85,86,87,88,89,90,91,92,93,94,95,96,97,98,99,100,101,102,103\n\tv_mov_b32 v\\i, 0\n\t.endr\n\t"
                "s_mov_b32 %[it], 0\n\ts_mov_b32 %[nx], 0x10000\n\t"
                "s_waitcnt lgkmcnt(0)\n\ts_barrier\n\t"
                "ds_read_b128 v[104:107], %[qa]\n\tds_read_b128 v[112:115], v129\n\t"
                "ds_read_b128 v[108:111], %[qa] offset:1024\n\tds_read_b128 v[116:119], v129 offset:1024\n\t"
                "s_waitcnt lgkmcnt(2)\n\t"
                "v_mfma_f32_32x32x16_bf16 v[40:55], v[112:115], v[104:107], 0\n\t"
                "ds_read_b128 v[112:115], v129 offset:4096\n\t"
                "s_waitcnt lgkmcnt(1)\n\t"
                "v_mfma_f32_32x32x16_bf16 v[40:55], v[116:119], v[108:111], v[40:55]\n\t"
                "ds_read_b128 v[116:119], v129 offset:5120\n\t"
                "ds_read_b128 v[124:127], v129 offset:3072\n\t"
                "ds_read_b128 v[120:123], v129 offset:2048\n\t"
                "1:\n\t"
                "s_add_u32 %[nx], %[nx], 0x4000\n\ts_cmp_eq_u32 %[nx], 0x28000\n\ts_cselect_b32 %[nx], 0, %[nx]\n\t"
                "v_mov_b32 v130, v129\n\tv_add_u32 v129, %[nx], %[lb]\n\t"
                "s_cmp_lg_u32 %[it], 4\n\ts_cbranch_scc1 2f\n\ts_waitcnt vmcnt(0)\n\t2:\n\t"
                "s_barrier\n\t"
                HALF(40, 56, 0, v130, 3072, v130, 6144)
                HALF(56, 40, 4096, v130, 7168, v129, 2048)
                "s_add_u32 %[it], %[it], 1\n\ts_cmp_lg_u32 %[it], 16\n\ts_cbranch_scc1 1b\n\t"
                "s_waitcnt lgkmcnt(0)\n\t"
                "v_smfmac_f32_32x32x32_bf16 v[88:103], v[124:127], v[80:87], v128\n\t"
                "s_nop 12"
                : "={v[88:103]}"(o), "={v[104:107]}"(qf0), "={v[108:111]}"(qf1), [it] "=&s"(sit), [nx] "=&s"(snx)
                : [lb] "{v131}"(lb), [qa] "{v132}"(qa)
                : "memory", "scc", "v40", "v41", "v42", "v43", "v44", "v45", "v46", "v47", "v48", "v49", "v50", "v51", "v52", "v53", "v54", "v55",
                  "v56", "v57", "v58", "v59", "v60", "v61", "v62", "v63", "v64", "v65", "v66", "v67", "v68", "v69", "v70", "v71",
                  "v72", "v73", "v74", "v75", "v76", "v77", "v78", "v79", "v80", "v81", "v82", "v83", "v84", "v85", "v86", "v87",
                  "v112", "v113", "v114", "v115", "v116", "v117", "v118", "v119", "v120", "v121", "v122", "v123", "v124", "v125", "v126", "v127",
                  "v128", "v129", "v130");
        }
        STAMP(7);
        const u32 rsw = L_RS + w * 2048 + lane * 16, rsr = L_RS + (w ^ 4) * 2048 + lane * 16;
#define LO8(v) __builtin_shufflevector(v, v, 0, 1, 2, 3, 4, 5, 6, 7)
#define HI8(v) __builtin_shufflevector(v, v, 8, 9, 10, 11, 12, 13, 14, 15)
#define PAIR_PUT(v) do { keep = kh ? HI8(v) : LO8(v); const f32x8 gv_ = kh ? LO8(v) : HI8(v); \
            lds_st<f32x4>(rsw, __builtin_shufflevector(gv_, gv_, 0, 1, 2, 3)); lds_st<f32x4>(rsw + 1024, __builtin_shufflevector(gv_, gv_, 4, 5, 6, 7)); } while (0)
#define PAIR_GET() do { const f32x4 p0_ = lds_ld<f32x4>(rsr), p1_ = lds_ld<f32x4>(rsr + 1024); keep += __builtin_shufflevector(p0_, p1_, 0, 1, 2, 3, 4, 5, 6, 7); } while (0)
        f32x8 keep;
        PAIR_PUT(o);
        BAR();
        PAIR_GET();
        if (__builtin_expect(!__all(keep[7] > 0x1p-30f && keep[7] < 0x1p64f), 0)) {
            const f32x16 zero = {0};
            float m = -__builtin_inff();
#pragma unroll 1
            for (u32 t = 0; t < 64; ++t) {
                const u32 ko = W_KV + t * 4096 + lane * 16;
                const f32x16 s = MFMA32(ld128(rw, ko + 1024), qf1, MFMA32(ld128(rw, ko), qf0, zero));
#pragma unroll
                for (int r = 0; r < 16; ++r) m = fmaxf(m, s[r]);
            }
            m = fmaxf(m, __shfl_xor(m, 32));
            o = zero;
#pragma unroll 1
            for (u32 t = 0; t < 64; ++t) {
                const u32 ko = W_KV + t * 4096 + lane * 16;
                f32x16 s = MFMA32(ld128(rw, ko + 1024), qf1, MFMA32(ld128(rw, ko), qf0, zero));
#pragma unroll
                for (int r = 0; r < 16; ++r) s[r] = __builtin_amdgcn_exp2f(s[r] - m);
                o = __builtin_amdgcn_smfmac_f32_32x32x32_bf16(__builtin_bit_cast(bf16x8, ld128(rw, ko + 2048)), __builtin_bit_cast(bf16x16, LO8(s)), o, 0xdddd, 0, 0);
                o = __builtin_amdgcn_smfmac_f32_32x32x32_bf16(__builtin_bit_cast(bf16x8, ld128(rw, ko + 3072)), __builtin_bit_cast(bf16x16, HI8(s)), o, 0xdddd, 0, 0);
            }
            keep = kh ? HI8(o) : LO8(o);
        }
        {
            const float inv = __builtin_amdgcn_rcpf(keep[7]);
            const u32x4 f = {min(pk2(keep[0] * inv, keep[1] * inv), ~1u), min(pk2(keep[2] * inv, keep[3] * inv), ~1u),
                             min(pk2(keep[4] * inv, keep[5] * inv), ~1u), pk2(keep[6] * inv, 0.f)};
            const u32 ks = 8 * hg + 2 * hd + kh;
            st128(rw, W_OX + (qt * 32 + ks) * 1024 + lane * 16, f);
            lds_st<u32x4>(L_OS + ks * 1024 + lane * 16, f);
        }
        STAMP(8);
        const u32 k1 = ((8 * hg + 8 + w) & 31) * 1024 + lane * 16, k2 = ((8 * hg + 16 + w) & 31) * 1024 + lane * 16, k3 = ((8 * hg + 24 + w) & 31) * 1024 + lane * 16;
        const u32 oi = W_OX + qt * 32768;
        u32x4 wf[16];
        {
            const u32 cb = g4 & 1, q = (lane >> 2) & 3, p = lane & 3;
            const u32 col = 64 * hd + 32 * cb + 8 * (((p & 1) << 1) | (p >> 1));
            const u32 ra = 224 * kh + 7 * hh + q, rb = 224 * kh + 7 * hh + min(4 + q, 6u);
            const u32 wa = L_WO + ra * 256 + (col ^ ((ra & 3) << 6)), wb = L_WO + rb * 256 + (col ^ ((rb & 3) << 6));
#pragma unroll
            for (int i = 0; i < 16; ++i)
                wf[i] = cat(lds_tr((wa ^ ((i & 1) << 7)) + i * 3584), lds_tr((wb ^ ((i & 1) << 7)) + i * 3584));
        }
        u32x4 o1, o2, o3;
        for (int spin = 0; spin < SPIN_CAP; ++spin) {
            FENCE();
            o1 = ld128(rw, oi + k1); o2 = ld128(rw, oi + k2); o3 = ld128(rw, oi + k3);
            const u32 mx = max(max(max(max(o1.x, o1.y), max(o1.z, o1.w)), max(max(o2.x, o2.y), max(o2.z, o2.w))), max(max(o3.x, o3.y), max(o3.z, o3.w)));
            if (__all(mx != ~0u)) break;
        }
        lds_st<u32x4>(L_OS + k1, o1); lds_st<u32x4>(L_OS + k2, o2); lds_st<u32x4>(L_OS + k3, o3);
        STAMP(9);
        BAR();
        f32x16 acc = {0};
#pragma unroll
        for (int i = 0; i < 16; ++i) acc = MFMA32(wf[i], lds_ld<u32x4>(L_OS + (16 * kh + i) * 1024 + lane * 16), acc);
        PAIR_PUT(acc);
        STAMP(10);
        BAR();
        PAIR_GET();
        if (hg < 3 || hd < 2)
            *(u32x4*)(out + (32 * qt + r32) * 1792 + 256 * hg + 64 * hd + 32 * kh + 16 * hh) =
                (u32x4){pk2(keep[0], keep[1]), pk2(keep[2], keep[3]), pk2(keep[4], keep[5]), pk2(keep[6], keep[7])};
    }
}

extern "C" void kernel_launch(void* const* d_in, const int* in_sizes, int n_in, void* d_out, int out_size, void* d_ws, size_t ws_size,
                              hipStream_t stream) {
    fused_kernel<<<256, 768, 0, stream>>>((const char*)d_in[0], (const char*)d_in[1], (const char*)d_in[2], (const char*)d_in[3],
                                          (const char*)d_in[4], (const char*)d_in[5], (const u16*)d_in[6], (const u16*)d_in[7],
                                          (const u16*)d_in[8], (const u16*)d_in[9], (const char*)d_in[10], (const char*)d_in[11],
                                          (char*)d_out, (char*)d_ws);
}
```

```cpp
#define STAMPS
#define STAT 21
#include <hip/hip_runtime.h>
#include <stdint.h>

typedef unsigned short u16;
typedef unsigned u32;
typedef unsigned long long u64;
typedef u32 u32x2 __attribute__((ext_vector_type(2)));
typedef u32 u32x4 __attribute__((ext_vector_type(4)));
typedef float f32x2 __attribute__((ext_vector_type(2)));
typedef float f32x4 __attribute__((ext_vector_type(4)));
typedef float f32x8 __attribute__((ext_vector_type(8)));
typedef float f32x16 __attribute__((ext_vector_type(16)));
typedef __bf16 bf16x2 __attribute__((ext_vector_type(2)));
typedef __bf16 bf16x8 __attribute__((ext_vector_type(8)));
typedef __bf16 bf16x16 __attribute__((ext_vector_type(16)));
typedef short s16x4 __attribute__((ext_vector_type(4)));
#define LAS __attribute__((address_space(3)))
#define CAS __attribute__((address_space(4)))

constexpr u32 W_F1 = 4096;
constexpr u32 W_F2 = 16384;
constexpr u32 W_KV = 65536;
constexpr u32 W_QX = 1u << 20;
constexpr u32 W_UX = 5u << 20;
constexpr u32 W_YX = 9u << 20;
constexpr u32 W_OX = 11u << 20;
constexpr u32 W_END = 13u << 20;

constexpr u32 L_XS = 0;
constexpr u32 L_WS = 57344;
constexpr u32 L_SCR = 114688;
constexpr u32 L_US = 149504;
constexpr u32 L_KC = 159744;
constexpr u32 L_QS = 0;
constexpr u32 L_WO = 0;
constexpr u32 L_RS = 114688;
constexpr u32 L_OS = 131072;
constexpr u32 L_END = 163840;

constexpr int SPIN_CAP = 1 << 20;
constexpr float QSCALE = 0.18898223650461362f * 1.4426950408889634f;

#define BAR() asm volatile("s_waitcnt lgkmcnt(0)\n\ts_barrier" ::: "memory")
#define VMWAIT(n) asm volatile("s_waitcnt vmcnt(" #n ")" ::: "memory")
#define FENCE() asm volatile("" ::: "memory")
#define MFMA32(a, b, c) __builtin_amdgcn_mfma_f32_32x32x16_bf16(__builtin_bit_cast(bf16x8, a), __builtin_bit_cast(bf16x8, b), c, 0, 0, 0)
#define MFMA16(a, b, c) __builtin_amdgcn_mfma_f32_16x16x32_bf16(__builtin_bit_cast(bf16x8, a), __builtin_bit_cast(bf16x8, b), c, 0, 0, 0)

#define STAMP(i) do { u64 t_; asm volatile("s_memrealtime %0\n\ts_waitcnt lgkmcnt(0)" : "=s"(t_) :: "memory"); stamp_ = (lane == (i)) ? (u32)t_ : stamp_; } while (0)
#define CSTAMP(i) do { u64 t_; asm volatile("s_memtime %0\n\ts_waitcnt lgkmcnt(0)" : "=s"(t_) :: "memory"); stamp_ = (lane == (i)) ? (u32)t_ : stamp_; } while (0)

__device__ __forceinline__ float bf2f(u32 v) { return __uint_as_float(v << 16); }
__device__ __forceinline__ u32 pk2(float lo, float hi) {
    f32x2 v = {lo, hi};
    return __builtin_bit_cast(u32, __builtin_convertvector(v, bf16x2));
}
__device__ __forceinline__ float rbf(float f) { return bf2f(pk2(f, f) & 0xffffu); }
template <class T> __device__ __forceinline__ T lds_ld(u32 a) { return *(const LAS T*)(uintptr_t)a; }
template <class T> __device__ __forceinline__ void lds_st(u32 a, T v) { *(LAS T*)(uintptr_t)a = v; }
__device__ __forceinline__ u32x2 lds_tr(u32 a) {
    return __builtin_bit_cast(u32x2, __builtin_amdgcn_ds_read_tr16_b64_v4i16((LAS s16x4*)(uintptr_t)a));
}
__device__ __forceinline__ u32x4 cat(u32x2 a, u32x2 b) { return (u32x4){a.x, a.y, b.x, b.y}; }
__device__ __forceinline__ void dma(const char* g, u32 l) {
    asm volatile("s_mov_b32 m0, %1\n\ts_nop 0\n\tglobal_load_lds_dwordx4 %0, off" :: "v"(g), "s"(l) : "memory");
}
__device__ __forceinline__ void dma_dev(const char* g, u32 l) {
    asm volatile("s_mov_b32 m0, %1\n\ts_nop 0\n\tglobal_load_lds_dwordx4 %0, off sc1" :: "v"(g), "s"(l) : "memory");
}
typedef __amdgpu_buffer_rsrc_t rsrc_t;
__device__ __forceinline__ void st128(rsrc_t r, u32 off, u32x4 v) { __builtin_amdgcn_raw_buffer_store_b128(v, r, off, 0, 16); }
__device__ __forceinline__ void st32(rsrc_t r, u32 off, u32 v) { __builtin_amdgcn_raw_buffer_store_b32(v, r, off, 0, 16); }
__device__ __forceinline__ u32x4 ld128(rsrc_t r, u32 off) { return __builtin_amdgcn_raw_buffer_load_b128(r, off, 0, 16); }
__device__ __forceinline__ u32 ld32(rsrc_t r, u32 off) { return __builtin_amdgcn_raw_buffer_load_b32(r, off, 0, 16); }

__device__ __forceinline__ f32x2 cmul(f32x2 z, f32x2 w) {
    f32x2 o;
    asm("v_pk_mul_f32 %0, %1, %2 op_sel_hi:[1,0]\n\tv_pk_fma_f32 %0, %1, %2, %0 op_sel:[1,1,0] op_sel_hi:[0,1,1] neg_lo:[0,1,0]" : "=&v"(o) : "v"(z), "v"(w));
    return o;
}
__device__ __forceinline__ f32x2 cmulc(f32x2 z, f32x2 w) {
    f32x2 o;
    asm("v_pk_mul_f32 %0, %1, %2 op_sel_hi:[1,0]\n\tv_pk_fma_f32 %0, %1, %2, %0 op_sel:[1,1,0] op_sel_hi:[0,1,1] neg_hi:[0,1,0]" : "=&v"(o) : "v"(z), "v"(w));
    return o;
}
__device__ __forceinline__ f32x2 cexp_at(float a2, float rev, float p) {
    float e = __builtin_amdgcn_exp2f(a2 * p), r = __builtin_amdgcn_fractf(rev * p);
    return (f32x2){e * __builtin_amdgcn_cosf(r), e * __builtin_amdgcn_sinf(r)};
}

__global__ void __launch_bounds__(768, 3) fused_kernel(
    const char* __restrict__ x, const char* __restrict__ wq, const char* __restrict__ wk, const char* __restrict__ wv,
    const char* __restrict__ wo, const char* __restrict__ u, const u16* __restrict__ lar, const u16* __restrict__ aim,
    const u16* __restrict__ cre, const u16* __restrict__ cim, const char* __restrict__ ldt, const char* __restrict__ dsk,
    char* __restrict__ out, char* ws) {
    __shared__ __attribute__((aligned(1024))) char lds[L_END];
    asm volatile("" :: "s"((u32)(uintptr_t)lds));
    const u32 tid = threadIdx.x, lane = tid & 63, w = __builtin_amdgcn_readfirstlane(tid >> 6);
    const u32 g = blockIdx.x, xcd = g & 7, j = g >> 3;
    const bool kvwg = j < 4;
    const u32 jj = j - 4;
    const u32 rt = 4 * xcd + (kvwg ? j : jj / 7), cs = kvwg ? 7 : jj % 7;
    const u32 cp = 28 * xcd + jj;
    const u32 qt = 8 * xcd + (j >> 2), hg = j & 3;
    const u32 r32 = lane & 31, hh = lane >> 5, c16 = lane & 15, g4 = lane >> 4;
    u32 stamp_ = 0;
    STAMP(0);
    stamp_ = lane == 13 ? (u32)__builtin_amdgcn_s_getreg(63492) : stamp_;
    u32 tag = *(const CAS u32*)ws;
    const rsrc_t rw = __builtin_amdgcn_make_buffer_rsrc(ws, 0, W_END, 0x00020000);

    if (w >= 8) {
        const u32 lw = w - 8, rin = lane >> 3, pos = lane & 7;
        const char* xs = x + (64 * rt + 8 * lw + rin) * 896 + ((pos ^ ((4 * lw + g4) & 7)) << 4);
        const u32 chunk = pos ^ ((g4 & 1) << 2), cc = chunk & 3;
        const char* wsrc = kvwg ? ((chunk & 4) ? wv : wk) + (8 * lw + rin) * 56 + (cc < 3 ? 16 * cc : 40)
                                : wq + (8 * lw + rin) * 896 + 128 * cs + 16 * chunk;
        const u32 wp = kvwg ? 56 : 896;
        u32 a = (cs + 2 * (rt & 3) + xcd) % 7;
#pragma unroll 1
        for (int i = 0; i < 7; ++i) {
            const u32 l = a * 8192 + lw * 1024;
            dma(xs + a * 128, L_XS + l);
            dma(wsrc + a * 64 * wp, L_WS + l);
            dma(xs + a * 128 + 32 * 896, L_XS + l + 4096);
            dma(wsrc + (a * 64 + 32) * wp, L_WS + l + 4096);
            a = a == 6 ? 0 : a + 1;
        }
        asm volatile("" : "+s"(tag));
        tag = tag * 1664525u + 1013904223u;
        STAMP(1);
        VMWAIT(0);
    } else {
        if (!kvwg) {
            const char* us = u + (64 * rt + 2 * r32) * 896 + 128 * cs + 16 * w + 8 * hh;
            const u32x2 ua = *(const u32x2*)us, ub = *(const u32x2*)(us + 896);
            const u32 ch = w >> 2, pi = (2 * cp + ch) * 256 + 64 * (w & 3) + lane;
            const u32 vlar = lar[pi], vaim = aim[pi], vcr = cre[pi], vci = cim[pi];
            const u32 ldt2 = *(const CAS u32*)(ldt + 4 * cp);
            if (tid < 128) lds_st<u32x4>(L_US + (tid >> 4) * 1280 + (tid & 15) * 16, (u32x4){0, 0, 0, 0});
            if (tid < 2) lds_st<u32x4>(L_KC + tid * 1552, (u32x4){0, 0, 0, 0});
            asm volatile("" : "+s"(tag));
            tag = tag * 1664525u + 1013904223u;
            const u32 uo = W_UX + (32 * cs + 4 * w + 2 * hh) * 16384 + (32 * rt + r32) * 16;
            st128(rw, uo, (u32x4){__builtin_amdgcn_perm(ub.x, ua.x, 0x05040100), tag, __builtin_amdgcn_perm(ub.x, ua.x, 0x07060302), tag});
            st128(rw, uo + 16384, (u32x4){__builtin_amdgcn_perm(ub.y, ua.y, 0x05040100), tag, __builtin_amdgcn_perm(ub.y, ua.y, 0x07060302), tag});
            STAMP(1);
            const float dt = rbf(expf(bf2f(ch ? ldt2 >> 16 : ldt2 & 0xffffu)));
            const float a2 = -rbf(expf(bf2f(vlar))) * dt * 1.4426950408889634f;
            const float rev = bf2f(vaim) * dt * 0.15915494309189535f;
            const f32x2 w1 = cexp_at(a2, rev, 1.f), w8 = cexp_at(a2, rev, 8.f), w128 = cexp_at(a2, rev, 128.f);
            const u32 sw = L_SCR + w * 4352 + lane * 4, sr = L_SCR + w * 4352 + c16 * 272 + g4 * 16;
#define PUT(row, z) lds_st<u32>(sw + (row) * 272, pk2((z).x, (z).y))
            u32x4 bf[4];
            {
                f32x2 ze = cmul(w8, w8), zo = w8;
                const f32x2 w16 = ze;
                lds_st<u32>(sw, 0x3f80u);
                PUT(1, zo);
#pragma unroll
                for (int i = 2; i < 16; i += 2) {
                    zo = cmul(zo, w16);
                    PUT(i, ze); PUT(i + 1, zo);
                    if (i < 14) ze = cmul(ze, w16);
                }
#pragma unroll
                for (int k = 0; k < 4; ++k) bf[k] = lds_ld<u32x4>(sr + 64 * k);
            }
            f32x4 acc[3];
#define TILE(t) do { acc[t] = (f32x4){0, 0, 0, 0}; _Pragma("unroll") for (int k = 0; k < 4; ++k) acc[t] = MFMA16(lds_ld<u32x4>(sr + 64 * k), bf[k], acc[t]); } while (0)
            f32x2 e[4];
            {
                f32x2 z = {2.f * (bf2f(vcr) * dt), -2.f * (bf2f(vci) * dt)};
#pragma unroll
                for (int i = 0; i < 8; ++i) {
                    const f32x2 y = cmulc(z, w128);
                    PUT(i, z); PUT(8 + i, y);
                    if (!(i & 1)) e[i >> 1] = y;
                    if (i < 7) z = cmulc(z, w1);
                }
            }
            TILE(0);
#pragma unroll
            for (int s = 0; s < 2; ++s)
#pragma unroll
                for (int i = 0; i < 4; ++i) { e[i] = cmulc(e[i], w128); PUT(4 * s + i, e[i]); }
#pragma unroll
            for (int s = 0; s < 4; ++s) { e[0] = cmulc(e[0], w128); e[2] = cmulc(e[2], w128); PUT(8 + 2 * s, e[0]); PUT(9 + 2 * s, e[2]); }
            TILE(1);
#pragma unroll
            for (int i = 0; i < 9; ++i) { e[0] = cmulc(e[0], w128); PUT(i, e[0]); }
            TILE(2);
#pragma unroll
            for (int t = 0; t < 3; ++t) lds_st<f32x4>(L_SCR + w * 4352 + t * 1024 + lane * 16, acc[t]);
        } else {
            asm volatile("" : "+s"(tag));
            tag = tag * 1664525u + 1013904223u;
        }
    }
    STAMP(2);
    BAR();
    STAMP(3);

    if (w < 4) {
        const u32 mt = w >> 1, nt = w & 1;
        const bool isv = kvwg && nt;
        const u32 xr = 32 * mt + (isv ? ((r32 & 19) | ((r32 & 4) << 1) | ((r32 & 8) >> 1)) : r32);
        const u32 cb = g4 & 1, q = (lane >> 2) & 3, p = lane & 3, ps = (kvwg && cb) ? p ^ (p >> 1) : p;
        u32 xa = L_XS + xr * 128 + ((hh ^ ((xr >> 1) & 7)) << 4);
        u32 wa = L_WS + (8 * hh + q) * 128 + ((64 * nt + 32 * cb + 8 * ps) ^ ((q >> 1) << 6));
        f32x16 acc = {0};
#pragma unroll 1
        for (int a = 0; a < 7; ++a) {
            u32x4 xf[4], wf[4];
#pragma unroll
            for (int b = 0; b < 4; ++b) {
                xf[b] = lds_ld<u32x4>(xa ^ (b << 5));
                wf[b] = cat(lds_tr(wa + b * 2048), lds_tr(wa + b * 2048 + 512));
            }
            if (isv) {
#pragma unroll
                for (int b = 0; b < 4; ++b) acc = MFMA32(xf[b], wf[b], acc);
            } else {
#pragma unroll
                for (int b = 0; b < 4; ++b) acc = MFMA32(wf[b], xf[b], acc);
            }
            xa += 8192; wa += 8192;
            if (a == 1) BAR();
        }
        STAMP(4);
        BAR();
        if (!kvwg) {
            const u32 qo = W_QX + (((2 * rt + mt) * 112 + 16 * cs + 8 * nt) * 32 + lane) * 16;
#pragma unroll
            for (int r = 0; r < 4; ++r)
                st128(rw, qo + r * 1024, (u32x4){pk2(acc[4 * r] * QSCALE, acc[4 * r + 1] * QSCALE), tag, pk2(acc[4 * r + 2] * QSCALE, acc[4 * r + 3] * QSCALE), tag});
        } else {
            u32x4 f0 = {pk2(acc[0], acc[1]), pk2(acc[2], acc[3]), pk2(acc[4], acc[5]), pk2(acc[6], acc[7])};
            u32x4 f1 = {pk2(acc[8], acc[9]), pk2(acc[10], acc[11]), pk2(acc[12], acc[13]), pk2(acc[14], acc[15])};
            u32 slot = lane;
            if (nt) {
                const u32 s = r32 / 14, rem = r32 - 14 * s, h2 = rem / 7, i = rem - 7 * h2;
                slot = 16 * s + 8 * (i >> 2) + 4 * h2 + (i & 3);
                if (r32 >= 28) {
                    slot = 11 + 4 * (r32 & 1) + 8 * (r32 & 2);
                    f0 = f1 = (u32x4){0x3f803f80u, 0x3f803f80u, 0x3f803f80u, 0x3f803f80u};
                }
                slot += 32 * hh;
            } else if (hh) {
                f1.z = 0; f1.w = 0;
            }
            const u32 ko = W_KV + (2 * rt + mt) * 4096 + nt * 2048 + slot * 16;
            st128(rw, ko, f0);
            st128(rw, ko + 1024, f1);
            VMWAIT(0);
            BAR();
        }
        const u32 oo = W_OX + (qt * 32 + 8 * hg + 2 * w) * 1024 + lane * 16;
        st128(rw, oo, (u32x4){~0u, ~0u, ~0u, ~0u});
        st128(rw, oo + 1024, (u32x4){~0u, ~0u, ~0u, ~0u});
        STAMP(5);
        const u32 qi = W_QX + ((qt * 112 + 7 * (4 * hg + w)) * 32 + lane) * 16;
        u32x4 q0, q1, q2, q3;
        for (int spin = 0; spin < SPIN_CAP; ++spin) {
            FENCE();
            q0 = ld128(rw, qi); q1 = ld128(rw, qi + 1024); q2 = ld128(rw, qi + 2048); q3 = ld128(rw, qi + 3072 - 512 * hh);
            const bool ok = q0.y == tag && q0.w == tag && q1.y == tag && q1.w == tag && q2.y == tag && q2.w == tag && q3.y == tag && q3.w == tag;
            if (__all(ok)) break;
        }
        lds_st<u32x4>(L_QS + w * 2048 + lane * 16, (u32x4){q0.x, q0.z, q1.x, q1.z});
        lds_st<u32x4>(L_QS + w * 2048 + 1024 + lane * 16, (u32x4){q2.x, q2.z, hh ? 0u : q3.x, hh ? 0u : q3.z});
    } else {
        if (!kvwg) {
            const u32 t2 = tid - 256;
            const u32 ui = W_UX + cp * 16384 + t2 * 16;
            u32x4 u0 = ld128(rw, ui), u1 = ld128(rw, ui + 8192);
            if (w < 10) {
                const u32 c = (w - 4) / 3, t = (w - 4) - 3 * c;
                const u32 pa = L_SCR + c * 4 * 4352 + t * 1024 + lane * 16;
                f32x4 s = (lds_ld<f32x4>(pa) + lds_ld<f32x4>(pa + 4352)) + (lds_ld<f32x4>(pa + 2 * 4352) + lds_ld<f32x4>(pa + 3 * 4352));
                const u32 dsk2 = *(const CAS u32*)(dsk + 4 * cp);
                if (t == 0 && lane == 0) s.x += bf2f(c ? dsk2 >> 16 : dsk2 & 0xffffu);
                u32 base, s1 = 1, s2 = 2;
                if (t == 0) base = 128 * (g4 >> 1) + 8 * c16 + 4 * (g4 & 1);
                else if (t == 1) { base = 256 + 64 * g4 + (g4 < 2 ? 4 : 2) * c16; s2 = g4 < 2 ? 2 : 32; }
                else { base = 512 + 64 * g4 + c16; s1 = 16; s2 = 32; }
                const u32 ka = L_KC + c * 1552 + 16 + 2 * base, lo = pk2(s.x, s.y), hi = pk2(s.z, s.w);
                lds_st<u16>(ka, lo); lds_st<u16>(ka + 2 * s1, lo >> 16);
                lds_st<u16>(ka + 2 * s2, hi); lds_st<u16>(ka + 2 * (s1 + s2), hi >> 16);
            }
            BAR();
            {
                const u32 c = t2 >> 8, m = t2 & 255;
                const int o = max(0, 131 - 8 * (int)m);
                const float f0 = (float)(8 * (int)m - 3 + o);
                const u32 kb = L_KC + c * 1552 + 16 - 2 * (17024 + o);
                u32 kv[12];
#pragma unroll
                for (int e = 0; e < 12; e += 2) {
                    const u32 b = pk2(f0 + e, f0 + (e + 1));
                    kv[e] = lds_ld<u16>(kb + 2 * (b & 0xffffu));
                    if (e < 10) kv[e + 1] = lds_ld<u16>(kb + 2 * (b >> 16));
                }
                const u32 ta = L_SCR + c * 16640 + (m + 4) * 8;
#pragma unroll
                for (int i = 0; i < 8; ++i) lds_st<u32x2>(ta + i * 2080, (u32x2){kv[i + 3] | (kv[i + 2] << 16), kv[i + 1] | (kv[i] << 16)});
                if (m < 32) lds_st<u32x2>(L_SCR + c * 16640 + (m >> 2) * 2080 + (m & 3) * 8, (u32x2){0, 0});
            }
            for (int spin = 0; spin < SPIN_CAP; ++spin) {
                if (__all(u0.y == tag && u0.w == tag && u1.y == tag && u1.w == tag)) break;
                FENCE();
                u0 = ld128(rw, ui); u1 = ld128(rw, ui + 8192);
            }
            const u32 ua = L_US + ((t2 >> 2) & 3) * 1280 + (16 + (t2 >> 4)) * 16 + (t2 & 3) * 4;
            lds_st<u32>(ua, u0.x); lds_st<u32>(ua + 5120, u0.z);
            lds_st<u32>(ua + 512, u1.x); lds_st<u32>(ua + 5120 + 512, u1.z);
            STAMP(4);
            BAR();
        } else {
            BAR();
            BAR();
            BAR();
            if (tid == 512) st32(rw, W_F1 + rt * 128, tag);
        }
        if (w < 8) {
            if (!kvwg) {
                const u32 c = (w >> 1) & 1, al = w & 1;
                const u32 x0 = 32 + 16 * al + c16 - 8 * g4, x1 = x0 - 4;
                u32 a0 = L_SCR + c * 16640 + (x0 & 7) * 2080 + (x0 >> 3) * 8;
                u32 a1 = L_SCR + c * 16640 + (x1 & 7) * 2080 + (x1 >> 3) * 8;
                u32 ba = L_US + c * 5120 + g4 * 1280 + (16 + c16) * 16;
                f32x4 y[4] = {{0, 0, 0, 0}, {0, 0, 0, 0}, {0, 0, 0, 0}, {0, 0, 0, 0}};
                u32x4 A[4], B[4], An[4], Bn[4];
#define CLOAD(A_, B_, r) _Pragma("unroll") for (int s = 0; s < 4; ++s) { \
                    A_[s] = cat(lds_ld<u32x2>(a0 + 32 * (r) + 512 * s), lds_ld<u32x2>(a1 + 32 * (r) + 512 * s)); B_[s] = lds_ld<u32x4>(ba + 16 - 16 * (r) + 256 * s); }
#define CMMA(A_, B_) _Pragma("unroll") for (int s = 0; s < 4; ++s) _Pragma("unroll") for (int i = s; i < 4; ++i) y[i] = MFMA16(A_[s], B_[i - s], y[i]);
                ba -= 16;
                CLOAD(A, B, 0);
#pragma unroll 1
                for (int r = 0; r < 8; ++r) {
                    CLOAD(An, Bn, 1);
                    CMMA(A, B);
                    a0 += 64; a1 += 64; ba -= 32;
                    CLOAD(A, B, 0);
                    CMMA(An, Bn);
                }
                const u32 d = 2 * cp + c;
                const u32 yo = W_YX + (((d >> 6) * 32 + 2 * c16 + al) * 64 + (d & 63)) * 128 + g4 * 16;
                st128(rw, yo, (u32x4){pk2(y[0].x, y[0].y), pk2(y[0].z, y[0].w), pk2(y[1].x, y[1].y), pk2(y[1].z, y[1].w)});
                st128(rw, yo + 64, (u32x4){pk2(y[2].x, y[2].y), pk2(y[2].z, y[2].w), pk2(y[3].x, y[3].y), pk2(y[3].z, y[3].w)});
            }
        }
    }

    if (w >= 8) {
        const u32 lw = w - 8;
        for (int spin = 0; spin < SPIN_CAP; ++spin) {
            FENCE();
            if (__all(ld32(rw, W_F1 + r32 * 128) == tag)) break;
        }
        STAMP(5);
        const char* kvs = ws + W_KV + lw * 4096 + lane * 16;
        const u32 colb = (c16 ^ (g4 << 2)) << 4;
        const char* wos = wo + (16 * lw + g4) * 896 + 256 * hg + (hg == 3 ? colb & 127 : colb);
#define KVSTAGE(n, slot) _Pragma("unroll") for (int i = 0; i < 4; ++i) dma_dev(kvs + (n) * 16384 + i * 1024, (slot) * 16384 + lw * 4096 + i * 1024);
        KVSTAGE(0, 4); KVSTAGE(1, 5); KVSTAGE(2, 6);
        VMWAIT(8);
        STAMP(6);
        BAR();
        STAMP(9); CSTAMP(14);
        KVSTAGE(3, 7);
        const u32 ycp = 32 * cs + r32, yx = ycp / 28;
        const u32 fw = hh ? xcd + 8 * ((j & ~3u) + (lane & 3)) : (kvwg ? g : yx + 8 * (ycp - 28 * yx + 4));
        u32 f2 = 0, slot = 8;
#pragma unroll 1
        for (u32 it = 0; it < 16; ++it) {
            VMWAIT(8);
            asm volatile("s_barrier" ::: "memory");
            if (it < 12) { KVSTAGE(it + 4, slot); slot = slot == 9 ? 0 : slot + 1; }
            if (it >= 8 && it < 15) {
#pragma unroll
                for (int i = 0; i < 4; ++i) dma(wos + ((it - 8) * 64 + 4 * i) * 896, L_WO + (it - 8) * 16384 + lw * 4096 + i * 1024);
            }
            if (it == 4 && tid == 512) st32(rw, W_F2 + g * 128, tag);
            if (it == 12) asm volatile("global_load_dword %0, %1, off sc1" : "+v"(f2) : "v"(ws + W_F2 + fw * 128) : "memory");
        }
        STAMP(10); CSTAMP(15);
        asm volatile("s_waitcnt vmcnt(0)" : "+v"(f2) :: "memory");
        for (int spin = 0; spin < SPIN_CAP; ++spin) {
            if (__all(f2 == tag)) break;
            FENCE();
            f2 = ld32(rw, W_F2 + fw * 128);
        }
        STAMP(7);
        BAR();
        if (!kvwg) {
            const u32 pos = 2 * lw + hh, yi = W_YX + ((cs * 32 + rt) * 64 + 2 * r32) * 128 + pos * 16;
            const u32x4 ya = ld128(rw, yi), yb = ld128(rw, yi + 128);
            char* yo = out + (16 * rt + 4 * (pos & 3) + 1024 * (pos >> 2)) * 1792 + 896 + 128 * cs + 4 * r32;
#pragma unroll
            for (int r = 0; r < 8; ++r)
                *(u32*)(yo + ((r & 3) + 512 * (r >> 2)) * 1792) = __builtin_amdgcn_perm(yb[r >> 1], ya[r >> 1], (r & 1) ? 0x07060302 : 0x05040100);
        } else if (g == 0 && lw == 0) {
            for (int spin = 0; spin < SPIN_CAP; ++spin) {
                FENCE();
                bool ok = true;
#pragma unroll
                for (int i = 0; i < 4; ++i) ok &= ld32(rw, W_F2 + (lane + 64 * i) * 128) == tag;
                if (__all(ok)) break;
            }
            if (lane == 0) st32(rw, 0, tag);
        }
        STAMP(8);
        BAR();
        BAR();
    } else {
        const u32 hd = w & 3, kh = w >> 2;
        STAMP(6);
        f32x16 o;
        u32x4 qf0, qf1;
        {
            const u32 lb = lane * 16 + kh * 8192, qa = L_QS + hd * 2048 + lane * 16;
#define E4(d, s) "v_exp_f32 v[" #d "+0], v[" #s "+0]\n\tv_exp_f32 v[" #d "+1], v[" #s "+1]\n\tv_exp_f32 v[" #d "+2], v[" #s "+2]\n\tv_exp_f32 v[" #d "+3], v[" #s "+3]\n\t"
#define HALF(S, SN, KO, V1B, V1O, V0B, V0O) \
            "s_waitcnt lgkmcnt(3)\n\tv_mfma_f32_32x32x16_bf16 v[" #SN ":" #SN "+15], v[112:115], v[104:107], 0\n\t" \
            "ds_read_b128 v[112:115], v129 offset:" #KO "\n\t" E4(72, S) \
            "s_waitcnt lgkmcnt(3)\n\tv_mfma_f32_32x32x16_bf16 v[" #SN ":" #SN "+15], v[116:119], v[108:111], v[" #SN ":" #SN "+15]\n\t" \
            "ds_read_b128 v[116:119], v129 offset:" #KO "+1024\n\t" E4(76, S+4) \
            "s_waitcnt lgkmcnt(3)\n\tv_smfmac_f32_32x32x32_bf16 v[88:103], v[124:127], v[80:87], v128\n\t" \
            "ds_read_b128 v[124:127], " #V1B " offset:" #V1O "\n\t" E4(80, S+8) \
            "s_waitcnt lgkmcnt(3)\n\tv_smfmac_f32_32x32x32_bf16 v[88:103], v[120:123], v[72:79], v128\n\t" \
            "ds_read_b128 v[120:123], " #V0B " offset:" #V0O "\n\t" E4(84, S+12)
            u32 sit, snx;
            asm volatile(
                "v_add_u32 v129, 0x10000, %[lb]\n\t"
                "v_mov_b32 v128, 0xdddd\n\t"
                ".irp i,80,81,82,83,84,85,86,87,88,89,90,91,92,93,94,95,96,97,98,99,100,101,102,103\n\tv_mov_b32 v\\i, 0\n\t.endr\n\t"
                "s_mov_b32 %[it], 0\n\ts_mov_b32 %[nx], 0x10000\n\t"
                "s_waitcnt lgkmcnt(0)\n\ts_barrier\n\t"
                "ds_read_b128 v[104:107], %[qa]\n\tds_read_b128 v[112:115], v129\n\t"
                "ds_read_b128 v[108:111], %[qa] offset:1024\n\tds_read_b128 v[116:119], v129 offset:1024\n\t"
                "s_waitcnt lgkmcnt(2)\n\t"
                "v_mfma_f32_32x32x16_bf16 v[40:55], v[112:115], v[104:107], 0\n\t"
                "ds_read_b128 v[112:115], v129 offset:4096\n\t"
                "s_waitcnt lgkmcnt(1)\n\t"
                "v_mfma_f32_32x32x16_bf16 v[40:55], v[116:119], v[108:111], v[40:55]\n\t"
                "ds_read_b128 v[116:119], v129 offset:5120\n\t"
                "ds_read_b128 v[124:127], v129 offset:3072\n\t"
                "ds_read_b128 v[120:123], v129 offset:2048\n\t"
                "1:\n\t"
                "s_add_u32 %[nx], %[nx], 0x4000\n\ts_cmp_eq_u32 %[nx], 0x28000\n\ts_cselect_b32 %[nx], 0, %[nx]\n\t"
                "v_mov_b32 v130, v129\n\tv_add_u32 v129, %[nx], %[lb]\n\t"
                "s_cmp_lg_u32 %[it], 4\n\ts_cbranch_scc1 2f\n\ts_waitcnt vmcnt(0)\n\t2:\n\t"
                "s_barrier\n\t"
                HALF(40, 56, 0, v130, 3072, v130, 6144)
                HALF(56, 40, 4096, v130, 7168, v129, 2048)
                "s_add_u32 %[it], %[it], 1\n\ts_cmp_lg_u32 %[it], 16\n\ts_cbranch_scc1 1b\n\t"
                "s_waitcnt lgkmcnt(0)\n\t"
                "v_smfmac_f32_32x32x32_bf16 v[88:103], v[124:127], v[80:87], v128\n\t"
                "s_nop 12"
                : "={v[88:103]}"(o), "={v[104:107]}"(qf0), "={v[108:111]}"(qf1), [it] "=&s"(sit), [nx] "=&s"(snx)
                : [lb] "{v131}"(lb), [qa] "{v132}"(qa)
                : "memory", "scc", "v40", "v41", "v42", "v43", "v44", "v45", "v46", "v47", "v48", "v49", "v50", "v51", "v52", "v53", "v54", "v55",
                  "v56", "v57", "v58", "v59", "v60", "v61", "v62", "v63", "v64", "v65", "v66", "v67", "v68", "v69", "v70", "v71",
                  "v72", "v73", "v74", "v75", "v76", "v77", "v78", "v79", "v80", "v81", "v82", "v83", "v84", "v85", "v86", "v87",
                  "v112", "v113", "v114", "v115", "v116", "v117", "v118", "v119", "v120", "v121", "v122", "v123", "v124", "v125", "v126", "v127",
                  "v128", "v129", "v130");
        }
        STAMP(7);
        const u32 rsw = L_RS + w * 2048 + lane * 16, rsr = L_RS + (w ^ 4) * 2048 + lane * 16;
#define LO8(v) __builtin_shufflevector(v, v, 0, 1, 2, 3, 4, 5, 6, 7)
#define HI8(v) __builtin_shufflevector(v, v, 8, 9, 10, 11, 12, 13, 14, 15)
#define PAIR_PUT(v) do { keep = kh ? HI8(v) : LO8(v); const f32x8 gv_ = kh ? LO8(v) : HI8(v); \
            lds_st<f32x4>(rsw, __builtin_shufflevector(gv_, gv_, 0, 1, 2, 3)); lds_st<f32x4>(rsw + 1024, __builtin_shufflevector(gv_, gv_, 4, 5, 6, 7)); } while (0)
#define PAIR_GET() do { const f32x4 p0_ = lds_ld<f32x4>(rsr), p1_ = lds_ld<f32x4>(rsr + 1024); keep += __builtin_shufflevector(p0_, p1_, 0, 1, 2, 3, 4, 5, 6, 7); } while (0)
        f32x8 keep;
        PAIR_PUT(o);
        BAR();
        PAIR_GET();
        if (__builtin_expect(!__all(keep[7] > 0x1p-30f && keep[7] < 0x1p64f), 0)) {
            const f32x16 zero = {0};
            float m = -__builtin_inff();
#pragma unroll 1
            for (u32 t = 0; t < 64; ++t) {
                const u32 ko = W_KV + t * 4096 + lane * 16;
                const f32x16 s = MFMA32(ld128(rw, ko + 1024), qf1, MFMA32(ld128(rw, ko), qf0, zero));
#pragma unroll
                for (int r = 0; r < 16; ++r) m = fmaxf(m, s[r]);
            }
            m = fmaxf(m, __shfl_xor(m, 32));
            o = zero;
#pragma unroll 1
            for (u32 t = 0; t < 64; ++t) {
                const u32 ko = W_KV + t * 4096 + lane * 16;
                f32x16 s = MFMA32(ld128(rw, ko + 1024), qf1, MFMA32(ld128(rw, ko), qf0, zero));
#pragma unroll
                for (int r = 0; r < 16; ++r) s[r] = __builtin_amdgcn_exp2f(s[r] - m);
                o = __builtin_amdgcn_smfmac_f32_32x32x32_bf16(__builtin_bit_cast(bf16x8, ld128(rw, ko + 2048)), __builtin_bit_cast(bf16x16, LO8(s)), o, 0xdddd, 0, 0);
                o = __builtin_amdgcn_smfmac_f32_32x32x32_bf16(__builtin_bit_cast(bf16x8, ld128(rw, ko + 3072)), __builtin_bit_cast(bf16x16, HI8(s)), o, 0xdddd, 0, 0);
            }
            keep = kh ? HI8(o) : LO8(o);
        }
        {
            const float inv = __builtin_amdgcn_rcpf(keep[7]);
            const u32x4 f = {min(pk2(keep[0] * inv, keep[1] * inv), ~1u), min(pk2(keep[2] * inv, keep[3] * inv), ~1u),
                             min(pk2(keep[4] * inv, keep[5] * inv), ~1u), pk2(keep[6] * inv, 0.f)};
            const u32 ks = 8 * hg + 2 * hd + kh;
            st128(rw, W_OX + (qt * 32 + ks) * 1024 + lane * 16, f);
            lds_st<u32x4>(L_OS + ks * 1024 + lane * 16, f);
        }
        STAMP(8);
        const u32 k1 = ((8 * hg + 8 + w) & 31) * 1024 + lane * 16, k2 = ((8 * hg + 16 + w) & 31) * 1024 + lane * 16, k3 = ((8 * hg + 24 + w) & 31) * 1024 + lane * 16;
        const u32 oi = W_OX + qt * 32768;
        u32x4 wf[16];
        {
            const u32 cb = g4 & 1, q = (lane >> 2) & 3, p = lane & 3;
            const u32 col = 64 * hd + 32 * cb + 8 * (((p & 1) << 1) | (p >> 1));
            const u32 ra = 224 * kh + 7 * hh + q, rb = 224 * kh + 7 * hh + min(4 + q, 6u);
            const u32 wa = L_WO + ra * 256 + (col ^ ((ra & 3) << 6)), wb = L_WO + rb * 256 + (col ^ ((rb & 3) << 6));
#pragma unroll
            for (int i = 0; i < 16; ++i)
                wf[i] = cat(lds_tr((wa ^ ((i & 1) << 7)) + i * 3584), lds_tr((wb ^ ((i & 1) << 7)) + i * 3584));
        }
        u32x4 o1, o2, o3;
        for (int spin = 0; spin < SPIN_CAP; ++spin) {
            FENCE();
            o1 = ld128(rw, oi + k1); o2 = ld128(rw, oi + k2); o3 = ld128(rw, oi + k3);
            const u32 mx = max(max(max(max(o1.x, o1.y), max(o1.z, o1.w)), max(max(o2.x, o2.y), max(o2.z, o2.w))), max(max(o3.x, o3.y), max(o3.z, o3.w)));
            if (__all(mx != ~0u)) break;
        }
        lds_st<u32x4>(L_OS + k1, o1); lds_st<u32x4>(L_OS + k2, o2); lds_st<u32x4>(L_OS + k3, o3);
        STAMP(9);
        BAR();
        f32x16 acc = {0};
#pragma unroll
        for (int i = 0; i < 16; ++i) acc = MFMA32(wf[i], lds_ld<u32x4>(L_OS + (16 * kh + i) * 1024 + lane * 16), acc);
        PAIR_PUT(acc);
        STAMP(10);
        BAR();
        PAIR_GET();
        if (hg < 3 || hd < 2)
            *(u32x4*)(out + (32 * qt + r32) * 1792 + 256 * hg + 64 * hd + 32 * kh + 16 * hh) =
                (u32x4){pk2(keep[0], keep[1]), pk2(keep[2], keep[3]), pk2(keep[4], keep[5]), pk2(keep[6], keep[7])};
    }
    STAMP(11);
    VMWAIT(0);
    STAMP(12);
    if (lane < 16) *(u32*)(ws + (32u << 20) + ((g * 12 + w) * 16 + lane) * 4) = stamp_;
}

__global__ void k_amp(const u32* st, u32 mul) {
    __shared__ u32 x0[8], mx, mn;
    const int g = threadIdx.x;
    if (g < 8) x0[g] = ~0u;
    if (g == 0) { mx = 0; }
    __syncthreads();
    u32 a = ~0u, b = 0;
    for (int w = 0; w < 12; ++w) { a = min(a, st[(g * 12 + w) * 16]); b = max(b, st[(g * 12 + w) * 16 + (STAT < 16 ? STAT : 12)]); }
    atomicMin(&x0[g & 7], a); atomicMax(&mx, b);
    __syncthreads();
    u32 last = 0, first = ~0u;
    for (int i = 0; i < 8; ++i) { last = max(last, x0[i]); first = min(first, x0[i]); }
    u32 v = STAT == 20 ? last - first : mx - last;
    if (STAT == 22) { const u32* q = st + (100 * 12 + 8) * 16; v = (q[15] - q[14]) * 10 / (q[10] - q[9]); }
    if (g < 64) { u64 d = (u64)min(v, 10000u) * mul, s, t;
        asm volatile("s_memrealtime %0\n s_waitcnt lgkmcnt(0)" : "=s"(s) :: "memory");
        do { asm volatile("s_memrealtime %0\n s_waitcnt lgkmcnt(0)" : "=s"(t) :: "memory"); } while (t - s < d); }
}

extern "C" void kernel_launch(void* const* d_in, const int* in_sizes, int n_in, void* d_out, int out_size, void* d_ws, size_t ws_size,
                              hipStream_t stream) {
    fused_kernel<<<256, 768, 0, stream>>>((const char*)d_in[0], (const char*)d_in[1], (const char*)d_in[2], (const char*)d_in[3],
                                          (const char*)d_in[4], (const char*)d_in[5], (const u16*)d_in[6], (const u16*)d_in[7],
                                          (const u16*)d_in[8], (const u16*)d_in[9], (const char*)d_in[10], (const char*)d_in[11],
                                          (char*)d_out, (char*)d_ws);
    k_amp<<<1, 256, 0, stream>>>((const u32*)((char*)d_ws + (32u << 20)), 0u);
}
```

```cpp
#include <hip/hip_runtime.h>
#include <stdint.h>

typedef unsigned short u16;
typedef unsigned u32;
typedef unsigned long long u64;
typedef u32 u32x2 __attribute__((ext_vector_type(2)));
typedef u32 u32x4 __attribute__((ext_vector_type(4)));
typedef float f32x2 __attribute__((ext_vector_type(2)));
typedef float f32x4 __attribute__((ext_vector_type(4)));
typedef float f32x8 __attribute__((ext_vector_type(8)));
typedef float f32x16 __attribute__((ext_vector_type(16)));
typedef __bf16 bf16x2 __attribute__((ext_vector_type(2)));
typedef __bf16 bf16x8 __attribute__((ext_vector_type(8)));
typedef __bf16 bf16x16 __attribute__((ext_vector_type(16)));
typedef short s16x4 __attribute__((ext_vector_type(4)));
#define LAS __attribute__((address_space(3)))
#define CAS __attribute__((address_space(4)))

constexpr u32 W_F1 = 4096;
constexpr u32 W_F2 = 16384;
constexpr u32 W_KV = 65536;
constexpr u32 W_QX = 1u << 20;
constexpr u32 W_UX = 5u << 20;
constexpr u32 W_YX = 9u << 20;
constexpr u32 W_OX = 11u << 20;
constexpr u32 W_END = 13u << 20;

constexpr u32 L_XS = 0;
constexpr u32 L_WS = 57344;
constexpr u32 L_SCR = 114688;
constexpr u32 L_US = 149504;
constexpr u32 L_KC = 159744;
constexpr u32 L_QS = 0;
constexpr u32 L_WO = 0;
constexpr u32 L_RS = 114688;
constexpr u32 L_OS = 131072;
constexpr u32 L_END = 163840;

constexpr int SPIN_CAP = 1 << 20;
constexpr float QSCALE = 0.18898223650461362f * 1.4426950408889634f;

#define BAR() asm volatile("s_waitcnt lgkmcnt(0)\n\ts_barrier" ::: "memory")
#define VMWAIT(n) asm volatile("s_waitcnt vmcnt(" #n ")" ::: "memory")
#define FENCE() asm volatile("" ::: "memory")
#define MFMA32(a, b, c) __builtin_amdgcn_mfma_f32_32x32x16_bf16(__builtin_bit_cast(bf16x8, a), __builtin_bit_cast(bf16x8, b), c, 0, 0, 0)
#define MFMA16(a, b, c) __builtin_amdgcn_mfma_f32_16x16x32_bf16(__builtin_bit_cast(bf16x8, a), __builtin_bit_cast(bf16x8, b), c, 0, 0, 0)

#define CSTAMP(i) do { } while (0)
#define STAMP(i) do { } while (0)

__device__ __forceinline__ float bf2f(u32 v) { return __uint_as_float(v << 16); }
__device__ __forceinline__ u32 pk2(float lo, float hi) {
    f32x2 v = {lo, hi};
    return __builtin_bit_cast(u32, __builtin_convertvector(v, bf16x2));
}
__device__ __forceinline__ float rbf(float f) { return bf2f(pk2(f, f) & 0xffffu); }
template <class T> __device__ __forceinline__ T lds_ld(u32 a) { return *(const LAS T*)(uintptr_t)a; }
template <class T> __device__ __forceinline__ void lds_st(u32 a, T v) { *(LAS T*)(uintptr_t)a = v; }
__device__ __forceinline__ u32x2 lds_tr(u32 a) {
    return __builtin_bit_cast(u32x2, __builtin_amdgcn_ds_read_tr16_b64_v4i16((LAS s16x4*)(uintptr_t)a));
}
__device__ __forceinline__ u32x4 cat(u32x2 a, u32x2 b) { return (u32x4){a.x, a.y, b.x, b.y}; }
__device__ __forceinline__ void dma(const char* g, u32 l) {
    asm volatile("s_mov_b32 m0, %1\n\ts_nop 0\n\tglobal_load_lds_dwordx4 %0, off" :: "v"(g), "s"(l) : "memory");
}
__device__ __forceinline__ void dma_dev(const char* g, u32 l) {
    asm volatile("s_mov_b32 m0, %1\n\ts_nop 0\n\tglobal_load_lds_dwordx4 %0, off sc1" :: "v"(g), "s"(l) : "memory");
}
typedef __amdgpu_buffer_rsrc_t rsrc_t;
__device__ __forceinline__ void st128(rsrc_t r, u32 off, u32x4 v) { __builtin_amdgcn_raw_buffer_store_b128(v, r, off, 0, 16); }
__device__ __forceinline__ void st32(rsrc_t r, u32 off, u32 v) { __builtin_amdgcn_raw_buffer_store_b32(v, r, off, 0, 16); }
__device__ __forceinline__ u32x4 ld128(rsrc_t r, u32 off) { return __builtin_amdgcn_raw_buffer_load_b128(r, off, 0, 16); }
__device__ __forceinline__ u32 ld32(rsrc_t r, u32 off) { return __builtin_amdgcn_raw_buffer_load_b32(r, off, 0, 16); }

__device__ __forceinline__ f32x2 cmul(f32x2 z, f32x2 w) {
    f32x2 o;
    asm("v_pk_mul_f32 %0, %1, %2 op_sel_hi:[1,0]\n\tv_pk_fma_f32 %0, %1, %2, %0 op_sel:[1,1,0] op_sel_hi:[0,1,1] neg_lo:[0,1,0]" : "=&v"(o) : "v"(z), "v"(w));
    return o;
}
__device__ __forceinline__ f32x2 cmulc(f32x2 z, f32x2 w) {
    f32x2 o;
    asm("v_pk_mul_f32 %0, %1, %2 op_sel_hi:[1,0]\n\tv_pk_fma_f32 %0, %1, %2, %0 op_sel:[1,1,0] op_sel_hi:[0,1,1] neg_hi:[0,1,0]" : "=&v"(o) : "v"(z), "v"(w));
    return o;
}
__device__ __forceinline__ f32x2 cexp_at(float a2, float rev, float p) {
    float e = __builtin_amdgcn_exp2f(a2 * p), r = __builtin_amdgcn_fractf(rev * p);
    return (f32x2){e * __builtin_amdgcn_cosf(r), e * __builtin_amdgcn_sinf(r)};
}

__global__ void __launch_bounds__(768, 3) fused_kernel(
    const char* __restrict__ x, const char* __restrict__ wq, const char* __restrict__ wk, const char* __restrict__ wv,
    const char* __restrict__ wo, const char* __restrict__ u, const u16* __restrict__ lar, const u16* __restrict__ aim,
    const u16* __restrict__ cre, const u16* __restrict__ cim, const char* __restrict__ ldt, const char* __restrict__ dsk,
    char* __restrict__ out, char* ws) {
    __shared__ __attribute__((aligned(1024))) char lds[L_END];
    asm volatile("" :: "s"((u32)(uintptr_t)lds));
    const u32 tid = threadIdx.x, lane = tid & 63, w = __builtin_amdgcn_readfirstlane(tid >> 6);
    const u32 g = blockIdx.x, xcd = g & 7, j = g >> 3;
    const bool kvwg = j < 4;
    const u32 jj = j - 4;
    const u32 rt = 4 * xcd + (kvwg ? j : jj / 7), cs = kvwg ? 7 : jj % 7;
    const u32 cp = 28 * xcd + jj;
    const u32 qt = 8 * xcd + (j >> 2), hg = j & 3;
    const u32 r32 = lane & 31, hh = lane >> 5, c16 = lane & 15, g4 = lane >> 4;
    STAMP(0);
    u32 tag = *(const CAS u32*)ws;
    const rsrc_t rw = __builtin_amdgcn_make_buffer_rsrc(ws, 0, W_END, 0x00020000);

    if (w >= 8) {
        const u32 lw = w - 8, rin = lane >> 3, pos = lane & 7;
        const char* xs = x + (64 * rt + 8 * lw + rin) * 896 + ((pos ^ ((4 * lw + g4) & 7)) << 4);
        const u32 chunk = pos ^ ((g4 & 1) << 2), cc = chunk & 3;
        const char* wsrc = kvwg ? ((chunk & 4) ? wv : wk) + (8 * lw + rin) * 56 + (cc < 3 ? 16 * cc : 40)
                                : wq + (8 * lw + rin) * 896 + 128 * cs + 16 * chunk;
        const u32 wp = kvwg ? 56 : 896;
        u32 a = (cs + 2 * (rt & 3) + xcd) % 7;
#pragma unroll 1
        for (int i = 0; i < 7; ++i) {
            const u32 l = a * 8192 + lw * 1024;
            dma(xs + a * 128, L_XS + l);
            dma(wsrc + a * 64 * wp, L_WS + l);
            dma(xs + a * 128 + 32 * 896, L_XS + l + 4096);
            dma(wsrc + (a * 64 + 32) * wp, L_WS + l + 4096);
            a = a == 6 ? 0 : a + 1;
        }
        asm volatile("" : "+s"(tag));
        tag = tag * 1664525u + 1013904223u;
        STAMP(1);
        VMWAIT(0);
    } else {
        if (!kvwg) {
            const char* us = u + (64 * rt + 2 * r32) * 896 + 128 * cs + 16 * w + 8 * hh;
            const u32x2 ua = *(const u32x2*)us, ub = *(const u32x2*)(us + 896);
            const u32 ch = w >> 2, pi = (2 * cp + ch) * 256 + 64 * (w & 3) + lane;
            const u32 vlar = lar[pi], vaim = aim[pi], vcr = cre[pi], vci = cim[pi];
            const u32 ldt2 = *(const CAS u32*)(ldt + 4 * cp);
            if (tid < 128) lds_st<u32x4>(L_US + (tid >> 4) * 1280 + (tid & 15) * 16, (u32x4){0, 0, 0, 0});
            if (tid < 2) lds_st<u32x4>(L_KC + tid * 1552, (u32x4){0, 0, 0, 0});
            asm volatile("" : "+s"(tag));
            tag = tag * 1664525u + 1013904223u;
            const u32 uo = W_UX + (32 * cs + 4 * w + 2 * hh) * 16384 + (32 * rt + r32) * 16;
            st128(rw, uo, (u32x4){__builtin_amdgcn_perm(ub.x, ua.x, 0x05040100), tag, __builtin_amdgcn_perm(ub.x, ua.x, 0x07060302), tag});
            st128(rw, uo + 16384, (u32x4){__builtin_amdgcn_perm(ub.y, ua.y, 0x05040100), tag, __builtin_amdgcn_perm(ub.y, ua.y, 0x07060302), tag});
            STAMP(1);
            const float dt = rbf(expf(bf2f(ch ? ldt2 >> 16 : ldt2 & 0xffffu)));
            const float a2 = -rbf(expf(bf2f(vlar))) * dt * 1.4426950408889634f;
            const float rev = bf2f(vaim) * dt * 0.15915494309189535f;
            const f32x2 w1 = cexp_at(a2, rev, 1.f), w8 = cexp_at(a2, rev, 8.f), w128 = cexp_at(a2, rev, 128.f);
            const u32 sw = L_SCR + w * 4352 + lane * 4, sr = L_SCR + w * 4352 + c16 * 272 + g4 * 16;
#define PUT(row, z) lds_st<u32>(sw + (row) * 272, pk2((z).x, (z).y))
            u32x4 bf[4];
            {
                f32x2 ze = cmul(w8, w8), zo = w8;
                const f32x2 w16 = ze;
                lds_st<u32>(sw, 0x3f80u);
                PUT(1, zo);
#pragma unroll
                for (int i = 2; i < 16; i += 2) {
                    zo = cmul(zo, w16);
                    PUT(i, ze); PUT(i + 1, zo);
                    if (i < 14) ze = cmul(ze, w16);
                }
#pragma unroll
                for (int k = 0; k < 4; ++k) bf[k] = lds_ld<u32x4>(sr + 64 * k);
            }
            f32x4 acc[3];
#define TILE(t) do { acc[t] = (f32x4){0, 0, 0, 0}; _Pragma("unroll") for (int k = 0; k < 4; ++k) acc[t] = MFMA16(lds_ld<u32x4>(sr + 64 * k), bf[k], acc[t]); } while (0)
            f32x2 e[4];
            {
                f32x2 z = {2.f * (bf2f(vcr) * dt), -2.f * (bf2f(vci) * dt)};
#pragma unroll
                for (int i = 0; i < 8; ++i) {
                    const f32x2 y = cmulc(z, w128);
                    PUT(i, z); PUT(8 + i, y);
                    if (!(i & 1)) e[i >> 1] = y;
                    if (i < 7) z = cmulc(z, w1);
                }
            }
            TILE(0);
#pragma unroll
            for (int s = 0; s < 2; ++s)
#pragma unroll
                for (int i = 0; i < 4; ++i) { e[i] = cmulc(e[i], w128); PUT(4 * s + i, e[i]); }
#pragma unroll
            for (int s = 0; s < 4; ++s) { e[0] = cmulc(e[0], w128); e[2] = cmulc(e[2], w128); PUT(8 + 2 * s, e[0]); PUT(9 + 2 * s, e[2]); }
            TILE(1);
#pragma unroll
            for (int i = 0; i < 9; ++i) { e[0] = cmulc(e[0], w128); PUT(i, e[0]); }
            TILE(2);
#pragma unroll
            for (int t = 0; t < 3; ++t) lds_st<f32x4>(L_SCR + w * 4352 + t * 1024 + lane * 16, acc[t]);
        } else {
            asm volatile("" : "+s"(tag));
            tag = tag * 1664525u + 1013904223u;
        }
    }
    STAMP(2);
    BAR();
    STAMP(3);

    if (w < 4) {
        const u32 mt = w >> 1, nt = w & 1;
        const bool isv = kvwg && nt;
        const u32 xr = 32 * mt + (isv ? ((r32 & 19) | ((r32 & 4) << 1) | ((r32 & 8) >> 1)) : r32);
        const u32 cb = g4 & 1, q = (lane >> 2) & 3, p = lane & 3, ps = (kvwg && cb) ? p ^ (p >> 1) : p;
        u32 xa = L_XS + xr * 128 + ((hh ^ ((xr >> 1) & 7)) << 4);
        u32 wa = L_WS + (8 * hh + q) * 128 + ((64 * nt + 32 * cb + 8 * ps) ^ ((q >> 1) << 6));
        f32x16 acc = {0};
#pragma unroll 1
        for (int a = 0; a < 7; ++a) {
            u32x4 xf[4], wf[4];
#pragma unroll
            for (int b = 0; b < 4; ++b) {
                xf[b] = lds_ld<u32x4>(xa ^ (b << 5));
                wf[b] = cat(lds_tr(wa + b * 2048), lds_tr(wa + b * 2048 + 512));
            }
            if (isv) {
#pragma unroll
                for (int b = 0; b < 4; ++b) acc = MFMA32(xf[b], wf[b], acc);
            } else {
#pragma unroll
                for (int b = 0; b < 4; ++b) acc = MFMA32(wf[b], xf[b], acc);
            }
            xa += 8192; wa += 8192;
            if (a == 1) BAR();
        }
        STAMP(4);
        BAR();
        if (!kvwg) {
            const u32 qo = W_QX + (((2 * rt + mt) * 112 + 16 * cs + 8 * nt) * 32 + lane) * 16;
#pragma unroll
            for (int r = 0; r < 4; ++r)
                st128(rw, qo + r * 1024, (u32x4){pk2(acc[4 * r] * QSCALE, acc[4 * r + 1] * QSCALE), tag, pk2(acc[4 * r + 2] * QSCALE, acc[4 * r + 3] * QSCALE), tag});
        } else {
            u32x4 f0 = {pk2(acc[0], acc[1]), pk2(acc[2], acc[3]), pk2(acc[4], acc[5]), pk2(acc[6], acc[7])};
            u32x4 f1 = {pk2(acc[8], acc[9]), pk2(acc[10], acc[11]), pk2(acc[12], acc[13]), pk2(acc[14], acc[15])};
            u32 slot = lane;
            if (nt) {
                const u32 s = r32 / 14, rem = r32 - 14 * s, h2 = rem / 7, i = rem - 7 * h2;
                slot = 16 * s + 8 * (i >> 2) + 4 * h2 + (i & 3);
                if (r32 >= 28) {
                    slot = 11 + 4 * (r32 & 1) + 8 * (r32 & 2);
                    f0 = f1 = (u32x4){0x3f803f80u, 0x3f803f80u, 0x3f803f80u, 0x3f803f80u};
                }
                slot += 32 * hh;
            } else if (hh) {
                f1.z = 0; f1.w = 0;
            }
            const u32 ko = W_KV + (2 * rt + mt) * 4096 + nt * 2048 + slot * 16;
            st128(rw, ko, f0);
            st128(rw, ko + 1024, f1);
            VMWAIT(0);
            BAR();
        }
        const u32 oo = W_OX + (qt * 32 + 8 * hg + 2 * w) * 1024 + lane * 16;
        st128(rw, oo, (u32x4){~0u, ~0u, ~0u, ~0u});
        st128(rw, oo + 1024, (u32x4){~0u, ~0u, ~0u, ~0u});
        STAMP(5);
        const u32 qi = W_QX + ((qt * 112 + 7 * (4 * hg + w)) * 32 + lane) * 16;
        u32x4 q0, q1, q2, q3;
        for (int spin = 0; spin < SPIN_CAP; ++spin) {
            FENCE();
            q0 = ld128(rw, qi); q1 = ld128(rw, qi + 1024); q2 = ld128(rw, qi + 2048); q3 = ld128(rw, qi + 3072 - 512 * hh);
            const bool ok = q0.y == tag && q0.w == tag && q1.y == tag && q1.w == tag && q2.y == tag && q2.w == tag && q3.y == tag && q3.w == tag;
            if (__all(ok)) break;
        }
        lds_st<u32x4>(L_QS + w * 2048 + lane * 16, (u32x4){q0.x, q0.z, q1.x, q1.z});
        lds_st<u32x4>(L_QS + w * 2048 + 1024 + lane * 16, (u32x4){q2.x, q2.z, hh ? 0u : q3.x, hh ? 0u : q3.z});
    } else {
        if (!kvwg) {
            const u32 t2 = tid - 256;
            const u32 ui = W_UX + cp * 16384 + t2 * 16;
            u32x4 u0 = ld128(rw, ui), u1 = ld128(rw, ui + 8192);
            if (w < 10) {
                const u32 c = (w - 4) / 3, t = (w - 4) - 3 * c;
                const u32 pa = L_SCR + c * 4 * 4352 + t * 1024 + lane * 16;
                f32x4 s = (lds_ld<f32x4>(pa) + lds_ld<f32x4>(pa + 4352)) + (lds_ld<f32x4>(pa + 2 * 4352) + lds_ld<f32x4>(pa + 3 * 4352));
                const u32 dsk2 = *(const CAS u32*)(dsk + 4 * cp);
                if (t == 0 && lane == 0) s.x += bf2f(c ? dsk2 >> 16 : dsk2 & 0xffffu);
                u32 base, s1 = 1, s2 = 2;
                if (t == 0) base = 128 * (g4 >> 1) + 8 * c16 + 4 * (g4 & 1);
                else if (t == 1) { base = 256 + 64 * g4 + (g4 < 2 ? 4 : 2) * c16; s2 = g4 < 2 ? 2 : 32; }
                else { base = 512 + 64 * g4 + c16; s1 = 16; s2 = 32; }
                const u32 ka = L_KC + c * 1552 + 16 + 2 * base, lo = pk2(s.x, s.y), hi = pk2(s.z, s.w);
                lds_st<u16>(ka, lo); lds_st<u16>(ka + 2 * s1, lo >> 16);
                lds_st<u16>(ka + 2 * s2, hi); lds_st<u16>(ka + 2 * (s1 + s2), hi >> 16);
            }
            BAR();
            {
                const u32 c = t2 >> 8, m = t2 & 255;
                const int o = max(0, 131 - 8 * (int)m);
                const float f0 = (float)(8 * (int)m - 3 + o);
                const u32 kb = L_KC + c * 1552 + 16 - 2 * (17024 + o);
                u32 kv[12];
#pragma unroll
                for (int e = 0; e < 12; e += 2) {
                    const u32 b = pk2(f0 + e, f0 + (e + 1));
                    kv[e] = lds_ld<u16>(kb + 2 * (b & 0xffffu));
                    if (e < 10) kv[e + 1] = lds_ld<u16>(kb + 2 * (b >> 16));
                }
                const u32 ta = L_SCR + c * 16640 + (m + 4) * 8;
#pragma unroll
                for (int i = 0; i < 8; ++i) lds_st<u32x2>(ta + i * 2080, (u32x2){kv[i + 3] | (kv[i + 2] << 16), kv[i + 1] | (kv[i] << 16)});
                if (m < 32) lds_st<u32x2>(L_SCR + c * 16640 + (m >> 2) * 2080 + (m & 3) * 8, (u32x2){0, 0});
            }
            for (int spin = 0; spin < SPIN_CAP; ++spin) {
                if (__all(u0.y == tag && u0.w == tag && u1.y == tag && u1.w == tag)) break;
                FENCE();
                u0 = ld128(rw, ui); u1 = ld128(rw, ui + 8192);
            }
            const u32 ua = L_US + ((t2 >> 2) & 3) * 1280 + (16 + (t2 >> 4)) * 16 + (t2 & 3) * 4;
            lds_st<u32>(ua, u0.x); lds_st<u32>(ua + 5120, u0.z);
            lds_st<u32>(ua + 512, u1.x); lds_st<u32>(ua + 5120 + 512, u1.z);
            STAMP(4);
            BAR();
        } else {
            BAR();
            BAR();
            BAR();
            if (tid == 512) st32(rw, W_F1 + rt * 128, tag);
        }
        if (w < 8) {
            if (!kvwg) {
                const u32 c = (w >> 1) & 1, al = w & 1;
                const u32 x0 = 32 + 16 * al + c16 - 8 * g4, x1 = x0 - 4;
                u32 a0 = L_SCR + c * 16640 + (x0 & 7) * 2080 + (x0 >> 3) * 8;
                u32 a1 = L_SCR + c * 16640 + (x1 & 7) * 2080 + (x1 >> 3) * 8;
                u32 ba = L_US + c * 5120 + g4 * 1280 + (16 + c16) * 16;
                f32x4 y[4] = {{0, 0, 0, 0}, {0, 0, 0, 0}, {0, 0, 0, 0}, {0, 0, 0, 0}};
                u32x4 A[4], B[4], An[4], Bn[4];
#define CLOAD(A_, B_, r) _Pragma("unroll") for (int s = 0; s < 4; ++s) { \
                    A_[s] = cat(lds_ld<u32x2>(a0 + 32 * (r) + 512 * s), lds_ld<u32x2>(a1 + 32 * (r) + 512 * s)); B_[s] = lds_ld<u32x4>(ba + 16 - 16 * (r) + 256 * s); }
#define CMMA(A_, B_) _Pragma("unroll") for (int s = 0; s < 4; ++s) _Pragma("unroll") for (int i = s; i < 4; ++i) y[i] = MFMA16(A_[s], B_[i - s], y[i]);
                ba -= 16;
                CLOAD(A, B, 0);
#pragma unroll 1
                for (int r = 0; r < 8; ++r) {
                    CLOAD(An, Bn, 1);
                    CMMA(A, B);
                    a0 += 64; a1 += 64; ba -= 32;
                    CLOAD(A, B, 0);
                    CMMA(An, Bn);
                }
                const u32 d = 2 * cp + c;
                const u32 yo = W_YX + (((d >> 6) * 32 + 2 * c16 + al) * 64 + (d & 63)) * 128 + g4 * 16;
                st128(rw, yo, (u32x4){pk2(y[0].x, y[0].y), pk2(y[0].z, y[0].w), pk2(y[1].x, y[1].y), pk2(y[1].z, y[1].w)});
                st128(rw, yo + 64, (u32x4){pk2(y[2].x, y[2].y), pk2(y[2].z, y[2].w), pk2(y[3].x, y[3].y), pk2(y[3].z, y[3].w)});
            }
        }
    }

    if (w >= 8) {
        const u32 lw = w - 8;
        for (int spin = 0; spin < SPIN_CAP; ++spin) {
            FENCE();
            if (__all(ld32(rw, W_F1 + r32 * 128) == tag)) break;
        }
        STAMP(5);
        const char* kvs = ws + W_KV + lw * 4096 + lane * 16;
        const u32 colb = (c16 ^ (g4 << 2)) << 4;
        const char* wos = wo + (16 * lw + g4) * 896 + 256 * hg + (hg == 3 ? colb & 127 : colb);
#define KVSTAGE(n, slot) _Pragma("unroll") for (int i = 0; i < 4; ++i) dma_dev(kvs + (n) * 16384 + i * 1024, (slot) * 16384 + lw * 4096 + i * 1024);
        KVSTAGE(0, 4); KVSTAGE(1, 5); KVSTAGE(2, 6);
        VMWAIT(8);
        STAMP(6);
        BAR();
        STAMP(9); CSTAMP(14);
        KVSTAGE(3, 7);
        const u32 ycp = 32 * cs + r32, yx = ycp / 28;
        const u32 fw = hh ? xcd + 8 * ((j & ~3u) + (lane & 3)) : (kvwg ? g : yx + 8 * (ycp - 28 * yx + 4));
        u32 f2 = 0, slot = 8;
#pragma unroll 1
        for (u32 it = 0; it < 16; ++it) {
            VMWAIT(8);
            asm volatile("s_barrier" ::: "memory");
            if (it < 12) { KVSTAGE(it + 4, slot); slot = slot == 9 ? 0 : slot + 1; }
            if (it >= 8 && it < 15) {
#pragma unroll
                for (int i = 0; i < 4; ++i) dma(wos + ((it - 8) * 64 + 4 * i) * 896, L_WO + (it - 8) * 16384 + lw * 4096 + i * 1024);
            }
            if (it == 4 && tid == 512) st32(rw, W_F2 + g * 128, tag);
            if (it == 12) asm volatile("global_load_dword %0, %1, off sc1" : "+v"(f2) : "v"(ws + W_F2 + fw * 128) : "memory");
        }
        STAMP(10); CSTAMP(15);
        asm volatile("s_waitcnt vmcnt(0)" : "+v"(f2) :: "memory");
        for (int spin = 0; spin < SPIN_CAP; ++spin) {
            if (__all(f2 == tag)) break;
            FENCE();
            f2 = ld32(rw, W_F2 + fw * 128);
        }
        STAMP(7);
        BAR();
        if (!kvwg) {
            const u32 pos = 2 * lw + hh, yi = W_YX + ((cs * 32 + rt) * 64 + 2 * r32) * 128 + pos * 16;
            const u32x4 ya = ld128(rw, yi), yb = ld128(rw, yi + 128);
            char* yo = out + (16 * rt + 4 * (pos & 3) + 1024 * (pos >> 2)) * 1792 + 896 + 128 * cs + 4 * r32;
#pragma unroll
            for (int r = 0; r < 8; ++r)
                *(u32*)(yo + ((r & 3) + 512 * (r >> 2)) * 1792) = __builtin_amdgcn_perm(yb[r >> 1], ya[r >> 1], (r & 1) ? 0x07060302 : 0x05040100);
        } else if (g == 0 && lw == 0) {
            for (int spin = 0; spin < SPIN_CAP; ++spin) {
                FENCE();
                bool ok = true;
#pragma unroll
                for (int i = 0; i < 4; ++i) ok &= ld32(rw, W_F2 + (lane + 64 * i) * 128) == tag;
                if (__all(ok)) break;
            }
            if (lane == 0) st32(rw, 0, tag);
        }
        STAMP(8);
        BAR();
        BAR();
    } else {
        const u32 hd = w & 3, kh = w >> 2;
        STAMP(6);
        f32x16 o;
        u32x4 qf0, qf1;
        {
            const u32 lb = lane * 16 + kh * 8192, qa = L_QS + hd * 2048 + lane * 16;
#define E4(d, s) "v_exp_f32 v[" #d "+0], v[" #s "+0]\n\tv_exp_f32 v[" #d "+1], v[" #s "+1]\n\tv_exp_f32 v[" #d "+2], v[" #s "+2]\n\tv_exp_f32 v[" #d "+3], v[" #s "+3]\n\t"
#define HALF(S, SN, KO, V1B, V1O, V0B, V0O) \
            "s_waitcnt lgkmcnt(3)\n\tv_mfma_f32_32x32x16_bf16 v[" #SN ":" #SN "+15], v[112:115], v[104:107], 0\n\t" \
            "ds_read_b128 v[112:115], v129 offset:" #KO "\n\t" E4(72, S) \
            "s_waitcnt lgkmcnt(3)\n\tv_mfma_f32_32x32x16_bf16 v[" #SN ":" #SN "+15], v[116:119], v[108:111], v[" #SN ":" #SN "+15]\n\t" \
            "ds_read_b128 v[116:119], v129 offset:" #KO "+1024\n\t" E4(76, S+4) \
            "s_waitcnt lgkmcnt(3)\n\tv_smfmac_f32_32x32x32_bf16 v[88:103], v[124:127], v[80:87], v128\n\t" \
            "ds_read_b128 v[124:127], " #V1B " offset:" #V1O "\n\t" E4(80, S+8) \
            "s_waitcnt lgkmcnt(3)\n\tv_smfmac_f32_32x32x32_bf16 v[88:103], v[120:123], v[72:79], v128\n\t" \
            "ds_read_b128 v[120:123], " #V0B " offset:" #V0O "\n\t" E4(84, S+12)
            u32 sit, snx;
            asm volatile(
                "v_add_u32 v129, 0x10000, %[lb]\n\t"
                "v_mov_b32 v128, 0xdddd\n\t"
                ".irp i,80,81,82,83,84,85,86,87,88,89,90,91,92,93,94,95,96,97,98,99,100,101,102,103\n\tv_mov_b32 v\\i, 0\n\t.endr\n\t"
                "s_mov_b32 %[it], 0\n\ts_mov_b32 %[nx], 0x10000\n\t"
                "s_waitcnt lgkmcnt(0)\n\ts_barrier\n\t"
                "ds_read_b128 v[104:107], %[qa]\n\tds_read_b128 v[112:115], v129\n\t"
                "ds_read_b128 v[108:111], %[qa] offset:1024\n\tds_read_b128 v[116:119], v129 offset:1024\n\t"
                "s_waitcnt lgkmcnt(2)\n\t"
                "v_mfma_f32_32x32x16_bf16 v[40:55], v[112:115], v[104:107], 0\n\t"
                "ds_read_b128 v[112:115], v129 offset:4096\n\t"
                "s_waitcnt lgkmcnt(1)\n\t"
                "v_mfma_f32_32x32x16_bf16 v[40:55], v[116:119], v[108:111], v[40:55]\n\t"
                "ds_read_b128 v[116:119], v129 offset:5120\n\t"
                "ds_read_b128 v[124:127], v129 offset:3072\n\t"
                "ds_read_b128 v[120:123], v129 offset:2048\n\t"
                "1:\n\t"
                "s_add_u32 %[nx], %[nx], 0x4000\n\ts_cmp_eq_u32 %[nx], 0x28000\n\ts_cselect_b32 %[nx], 0, %[nx]\n\t"
                "v_mov_b32 v130, v129\n\tv_add_u32 v129, %[nx], %[lb]\n\t"
                "s_cmp_lg_u32 %[it], 4\n\ts_cbranch_scc1 2f\n\ts_waitcnt vmcnt(0)\n\t2:\n\t"
                "s_barrier\n\t"
                HALF(40, 56, 0, v130, 3072, v130, 6144)
                HALF(56, 40, 4096, v130, 7168, v129, 2048)
                "s_add_u32 %[it], %[it], 1\n\ts_cmp_lg_u32 %[it], 16\n\ts_cbranch_scc1 1b\n\t"
                "s_waitcnt lgkmcnt(0)\n\t"
                "v_smfmac_f32_32x32x32_bf16 v[88:103], v[124:127], v[80:87], v128\n\t"
                "s_nop 12"
                : "={v[88:103]}"(o), "={v[104:107]}"(qf0), "={v[108:111]}"(qf1), [it] "=&s"(sit), [nx] "=&s"(snx)
                : [lb] "{v131}"(lb), [qa] "{v132}"(qa)
                : "memory", "scc", "v40", "v41", "v42", "v43", "v44", "v45", "v46", "v47", "v48", "v49", "v50", "v51", "v52", "v53", "v54", "v55",
                  "v56", "v57", "v58", "v59", "v60", "v61", "v62", "v63", "v64", "v65", "v66", "v67", "v68", "v69", "v70", "v71",
                  "v72", "v73", "v74", "v75", "v76", "v77", "v78", "v79", "v80", "v81", "v82", "v83", "v84", "v85", "v86", "v87",
                  "v112", "v113", "v114", "v115", "v116", "v117", "v118", "v119", "v120", "v121", "v122", "v123", "v124", "v125", "v126", "v127",
                  "v128", "v129", "v130");
        }
        STAMP(7);
        const u32 rsw = L_RS + w * 2048 + lane * 16, rsr = L_RS + (w ^ 4) * 2048 + lane * 16;
#define LO8(v) __builtin_shufflevector(v, v, 0, 1, 2, 3, 4, 5, 6, 7)
#define HI8(v) __builtin_shufflevector(v, v, 8, 9, 10, 11, 12, 13, 14, 15)
#define PAIR_PUT(v) do { keep = kh ? HI8(v) : LO8(v); const f32x8 gv_ = kh ? LO8(v) : HI8(v); \
            lds_st<f32x4>(rsw, __builtin_shufflevector(gv_, gv_, 0, 1, 2, 3)); lds_st<f32x4>(rsw + 1024, __builtin_shufflevector(gv_, gv_, 4, 5, 6, 7)); } while (0)
#define PAIR_GET() do { const f32x4 p0_ = lds_ld<f32x4>(rsr), p1_ = lds_ld<f32x4>(rsr + 1024); keep += __builtin_shufflevector(p0_, p1_, 0, 1, 2, 3, 4, 5, 6, 7); } while (0)
        f32x8 keep;
        PAIR_PUT(o);
        BAR();
        PAIR_GET();
        if (__builtin_expect(!__all(keep[7] > 0x1p-30f && keep[7] < 0x1p64f), 0)) {
            const f32x16 zero = {0};
            float m = -__builtin_inff();
#pragma unroll 1
            for (u32 t = 0; t < 64; ++t) {
                const u32 ko = W_KV + t * 4096 + lane * 16;
                const f32x16 s = MFMA32(ld128(rw, ko + 1024), qf1, MFMA32(ld128(rw, ko), qf0, zero));
#pragma unroll
                for (int r = 0; r < 16; ++r) m = fmaxf(m, s[r]);
            }
            m = fmaxf(m, __shfl_xor(m, 32));
            o = zero;
#pragma unroll 1
            for (u32 t = 0; t < 64; ++t) {
                const u32 ko = W_KV + t * 4096 + lane * 16;
                f32x16 s = MFMA32(ld128(rw, ko + 1024), qf1, MFMA32(ld128(rw, ko), qf0, zero));
#pragma unroll
                for (int r = 0; r < 16; ++r) s[r] = __builtin_amdgcn_exp2f(s[r] - m);
                o = __builtin_amdgcn_smfmac_f32_32x32x32_bf16(__builtin_bit_cast(bf16x8, ld128(rw, ko + 2048)), __builtin_bit_cast(bf16x16, LO8(s)), o, 0xdddd, 0, 0);
                o = __builtin_amdgcn_smfmac_f32_32x32x32_bf16(__builtin_bit_cast(bf16x8, ld128(rw, ko + 3072)), __builtin_bit_cast(bf16x16, HI8(s)), o, 0xdddd, 0, 0);
            }
            keep = kh ? HI8(o) : LO8(o);
        }
        {
            const float inv = __builtin_amdgcn_rcpf(keep[7]);
            const u32x4 f = {min(pk2(keep[0] * inv, keep[1] * inv), ~1u), min(pk2(keep[2] * inv, keep[3] * inv), ~1u),
                             min(pk2(keep[4] * inv, keep[5] * inv), ~1u), pk2(keep[6] * inv, 0.f)};
            const u32 ks = 8 * hg + 2 * hd + kh;
            st128(rw, W_OX + (qt * 32 + ks) * 1024 + lane * 16, f);
            lds_st<u32x4>(L_OS + ks * 1024 + lane * 16, f);
        }
        STAMP(8);
        const u32 k1 = ((8 * hg + 8 + w) & 31) * 1024 + lane * 16, k2 = ((8 * hg + 16 + w) & 31) * 1024 + lane * 16, k3 = ((8 * hg + 24 + w) & 31) * 1024 + lane * 16;
        const u32 oi = W_OX + qt * 32768;
        u32x4 wf[16];
        {
            const u32 cb = g4 & 1, q = (lane >> 2) & 3, p = lane & 3;
            const u32 col = 64 * hd + 32 * cb + 8 * (((p & 1) << 1) | (p >> 1));
            const u32 ra = 224 * kh + 7 * hh + q, rb = 224 * kh + 7 * hh + min(4 + q, 6u);
            const u32 wa = L_WO + ra * 256 + (col ^ ((ra & 3) << 6)), wb = L_WO + rb * 256 + (col ^ ((rb & 3) << 6));
#pragma unroll
            for (int i = 0; i < 16; ++i)
                wf[i] = cat(lds_tr((wa ^ ((i & 1) << 7)) + i * 3584), lds_tr((wb ^ ((i & 1) << 7)) + i * 3584));
        }
        u32x4 o1, o2, o3;
        for (int spin = 0; spin < SPIN_CAP; ++spin) {
            FENCE();
            o1 = ld128(rw, oi + k1); o2 = ld128(rw, oi + k2); o3 = ld128(rw, oi + k3);
            const u32 mx = max(max(max(max(o1.x, o1.y), max(o1.z, o1.w)), max(max(o2.x, o2.y), max(o2.z, o2.w))), max(max(o3.x, o3.y), max(o3.z, o3.w)));
            if (__all(mx != ~0u)) break;
        }
        lds_st<u32x4>(L_OS + k1, o1); lds_st<u32x4>(L_OS + k2, o2); lds_st<u32x4>(L_OS + k3, o3);
        STAMP(9);
        BAR();
        f32x16 acc = {0};
#pragma unroll
        for (int i = 0; i < 16; ++i) acc = MFMA32(wf[i], lds_ld<u32x4>(L_OS + (16 * kh + i) * 1024 + lane * 16), acc);
        PAIR_PUT(acc);
        STAMP(10);
        BAR();
        PAIR_GET();
        if (hg < 3 || hd < 2)
            *(u32x4*)(out + (32 * qt + r32) * 1792 + 256 * hg + 64 * hd + 32 * kh + 16 * hh) =
                (u32x4){pk2(keep[0], keep[1]), pk2(keep[2], keep[3]), pk2(keep[4], keep[5]), pk2(keep[6], keep[7])};
    }
}

extern "C" void kernel_launch(void* const* d_in, const int* in_sizes, int n_in, void* d_out, int out_size, void* d_ws, size_t ws_size,
                              hipStream_t stream) {
    fused_kernel<<<256, 768, 0, stream>>>((const char*)d_in[0], (const char*)d_in[1], (const char*)d_in[2], (const char*)d_in[3],
                                          (const char*)d_in[4], (const char*)d_in[5], (const u16*)d_in[6], (const u16*)d_in[7],
                                          (const u16*)d_in[8], (const u16*)d_in[9], (const char*)d_in[10], (const char*)d_in[11],
                                          (char*)d_out, (char*)d_ws);
}
```

```cpp
#define STAMPS
#define STAT 21
#include <hip/hip_runtime.h>
#include <stdint.h>

typedef unsigned short u16;
typedef unsigned u32;
typedef unsigned long long u64;
typedef u32 u32x2 __attribute__((ext_vector_type(2)));
typedef u32 u32x4 __attribute__((ext_vector_type(4)));
typedef float f32x2 __attribute__((ext_vector_type(2)));
typedef float f32x4 __attribute__((ext_vector_type(4)));
typedef float f32x8 __attribute__((ext_vector_type(8)));
typedef float f32x16 __attribute__((ext_vector_type(16)));
typedef __bf16 bf16x2 __attribute__((ext_vector_type(2)));
typedef __bf16 bf16x8 __attribute__((ext_vector_type(8)));
typedef __bf16 bf16x16 __attribute__((ext_vector_type(16)));
typedef short s16x4 __attribute__((ext_vector_type(4)));
#define LAS __attribute__((address_space(3)))
#define CAS __attribute__((address_space(4)))

constexpr u32 W_F1 = 4096;
constexpr u32 W_F2 = 16384;
constexpr u32 W_KV = 65536;
constexpr u32 W_QX = 1u << 20;
constexpr u32 W_UX = 5u << 20;
constexpr u32 W_YX = 9u << 20;
constexpr u32 W_OX = 11u << 20;
constexpr u32 W_END = 13u << 20;

constexpr u32 L_XS = 0;
constexpr u32 L_WS = 57344;
constexpr u32 L_SCR = 114688;
constexpr u32 L_US = 149504;
constexpr u32 L_KC = 159744;
constexpr u32 L_QS = 0;
constexpr u32 L_WO = 0;
constexpr u32 L_RS = 114688;
constexpr u32 L_OS = 131072;
constexpr u32 L_END = 163840;

constexpr int SPIN_CAP = 1 << 20;
constexpr float QSCALE = 0.18898223650461362f * 1.4426950408889634f;

#define BAR() asm volatile("s_waitcnt lgkmcnt(0)\n\ts_barrier" ::: "memory")
#define VMWAIT(n) asm volatile("s_waitcnt vmcnt(" #n ")" ::: "memory")
#define FENCE() asm volatile("" ::: "memory")
#define MFMA32(a, b, c) __builtin_amdgcn_mfma_f32_32x32x16_bf16(__builtin_bit_cast(bf16x8, a), __builtin_bit_cast(bf16x8, b), c, 0, 0, 0)
#define MFMA16(a, b, c) __builtin_amdgcn_mfma_f32_16x16x32_bf16(__builtin_bit_cast(bf16x8, a), __builtin_bit_cast(bf16x8, b), c, 0, 0, 0)

#define STAMP(i) do { u64 t_; asm volatile("s_memrealtime %0\n\ts_waitcnt lgkmcnt(0)" : "=s"(t_) :: "memory"); stamp_ = (lane == (i)) ? (u32)t_ : stamp_; } while (0)
#define CSTAMP(i) do { u64 t_; asm volatile("s_memtime %0\n\ts_waitcnt lgkmcnt(0)" : "=s"(t_) :: "memory"); stamp_ = (lane == (i)) ? (u32)t_ : stamp_; } while (0)

__device__ __forceinline__ float bf2f(u32 v) { return __uint_as_float(v << 16); }
__device__ __forceinline__ u32 pk2(float lo, float hi) {
    f32x2 v = {lo, hi};
    return __builtin_bit_cast(u32, __builtin_convertvector(v, bf16x2));
}
__device__ __forceinline__ float rbf(float f) { return bf2f(pk2(f, f) & 0xffffu); }
template <class T> __device__ __forceinline__ T lds_ld(u32 a) { return *(const LAS T*)(uintptr_t)a; }
template <class T> __device__ __forceinline__ void lds_st(u32 a, T v) { *(LAS T*)(uintptr_t)a = v; }
__device__ __forceinline__ u32x2 lds_tr(u32 a) {
    return __builtin_bit_cast(u32x2, __builtin_amdgcn_ds_read_tr16_b64_v4i16((LAS s16x4*)(uintptr_t)a));
}
__device__ __forceinline__ u32x4 cat(u32x2 a, u32x2 b) { return (u32x4){a.x, a.y, b.x, b.y}; }
__device__ __forceinline__ void dma(const char* g, u32 l) {
    asm volatile("s_mov_b32 m0, %1\n\ts_nop 0\n\tglobal_load_lds_dwordx4 %0, off" :: "v"(g), "s"(l) : "memory");
}
__device__ __forceinline__ void dma_dev(const char* g, u32 l) {
    asm volatile("s_mov_b32 m0, %1\n\ts_nop 0\n\tglobal_load_lds_dwordx4 %0, off sc1" :: "v"(g), "s"(l) : "memory");
}
typedef __amdgpu_buffer_rsrc_t rsrc_t;
__device__ __forceinline__ void st128(rsrc_t r, u32 off, u32x4 v) { __builtin_amdgcn_raw_buffer_store_b128(v, r, off, 0, 16); }
__device__ __forceinline__ void st32(rsrc_t r, u32 off, u32 v) { __builtin_amdgcn_raw_buffer_store_b32(v, r, off, 0, 16); }
__device__ __forceinline__ u32x4 ld128(rsrc_t r, u32 off) { return __builtin_amdgcn_raw_buffer_load_b128(r, off, 0, 16); }
__device__ __forceinline__ u32 ld32(rsrc_t r, u32 off) { return __builtin_amdgcn_raw_buffer_load_b32(r, off, 0, 16); }

__device__ __forceinline__ f32x2 cmul(f32x2 z, f32x2 w) {
    f32x2 o;
    asm("v_pk_mul_f32 %0, %1, %2 op_sel_hi:[1,0]\n\tv_pk_fma_f32 %0, %1, %2, %0 op_sel:[1,1,0] op_sel_hi:[0,1,1] neg_lo:[0,1,0]" : "=&v"(o) : "v"(z), "v"(w));
    return o;
}
__device__ __forceinline__ f32x2 cmulc(f32x2 z, f32x2 w) {
    f32x2 o;
    asm("v_pk_mul_f32 %0, %1, %2 op_sel_hi:[1,0]\n\tv_pk_fma_f32 %0, %1, %2, %0 op_sel:[1,1,0] op_sel_hi:[0,1,1] neg_hi:[0,1,0]" : "=&v"(o) : "v"(z), "v"(w));
    return o;
}
__device__ __forceinline__ f32x2 cexp_at(float a2, float rev, float p) {
    float e = __builtin_amdgcn_exp2f(a2 * p), r = __builtin_amdgcn_fractf(rev * p);
    return (f32x2){e * __builtin_amdgcn_cosf(r), e * __builtin_amdgcn_sinf(r)};
}

__global__ void __launch_bounds__(768, 3) fused_kernel(
    const char* __restrict__ x, const char* __restrict__ wq, const char* __restrict__ wk, const char* __restrict__ wv,
    const char* __restrict__ wo, const char* __restrict__ u, const u16* __restrict__ lar, const u16* __restrict__ aim,
    const u16* __restrict__ cre, const u16* __restrict__ cim, const char* __restrict__ ldt, const char* __restrict__ dsk,
    char* __restrict__ out, char* ws) {
    __shared__ __attribute__((aligned(1024))) char lds[L_END];
    asm volatile("" :: "s"((u32)(uintptr_t)lds));
    const u32 tid = threadIdx.x, lane = tid & 63, w = __builtin_amdgcn_readfirstlane(tid >> 6);
    const u32 g = blockIdx.x, xcd = g & 7, j = g >> 3;
    const bool kvwg = j < 4;
    const u32 jj = j - 4;
    const u32 rt = 4 * xcd + (kvwg ? j : jj / 7), cs = kvwg ? 7 : jj % 7;
    const u32 cp = 28 * xcd + jj;
    const u32 qt = 8 * xcd + (j >> 2), hg = j & 3;
    const u32 r32 = lane & 31, hh = lane >> 5, c16 = lane & 15, g4 = lane >> 4;
    u32 stamp_ = 0;
    STAMP(0);
    stamp_ = lane == 13 ? (u32)__builtin_amdgcn_s_getreg(63492) : stamp_;
    u32 tag = *(const CAS u32*)ws;
    const rsrc_t rw = __builtin_amdgcn_make_buffer_rsrc(ws, 0, W_END, 0x00020000);

    if (w >= 8) {
        const u32 lw = w - 8, rin = lane >> 3, pos = lane & 7;
        const char* xs = x + (64 * rt + 8 * lw + rin) * 896 + ((pos ^ ((4 * lw + g4) & 7)) << 4);
        const u32 chunk = pos ^ ((g4 & 1) << 2), cc = chunk & 3;
        const char* wsrc = kvwg ? ((chunk & 4) ? wv : wk) + (8 * lw + rin) * 56 + (cc < 3 ? 16 * cc : 40)
                                : wq + (8 * lw + rin) * 896 + 128 * cs + 16 * chunk;
        const u32 wp = kvwg ? 56 : 896;
        u32 a = (cs + 2 * (rt & 3) + xcd) % 7;
#pragma unroll 1
        for (int i = 0; i < 7; ++i) {
            const u32 l = a * 8192 + lw * 1024;
            dma(xs + a * 128, L_XS + l);
            dma(wsrc + a * 64 * wp, L_WS + l);
            dma(xs + a * 128 + 32 * 896, L_XS + l + 4096);
            dma(wsrc + (a * 64 + 32) * wp, L_WS + l + 4096);
            a = a == 6 ? 0 : a + 1;
        }
        asm volatile("" : "+s"(tag));
        tag = tag * 1664525u + 1013904223u;
        STAMP(1);
        VMWAIT(0);
    } else {
        if (!kvwg) {
            const char* us = u + (64 * rt + 2 * r32) * 896 + 128 * cs + 16 * w + 8 * hh;
            const u32x2 ua = *(const u32x2*)us, ub = *(const u32x2*)(us + 896);
            const u32 ch = w >> 2, pi = (2 * cp + ch) * 256 + 64 * (w & 3) + lane;
            const u32 vlar = lar[pi], vaim = aim[pi], vcr = cre[pi], vci = cim[pi];
            const u32 ldt2 = *(const CAS u32*)(ldt + 4 * cp);
            if (tid < 128) lds_st<u32x4>(L_US + (tid >> 4) * 1280 + (tid & 15) * 16, (u32x4){0, 0, 0, 0});
            if (tid < 2) lds_st<u32x4>(L_KC + tid * 1552, (u32x4){0, 0, 0, 0});
            asm volatile("" : "+s"(tag));
            tag = tag * 1664525u + 1013904223u;
            const u32 uo = W_UX + (32 * cs + 4 * w + 2 * hh) * 16384 + (32 * rt + r32) * 16;
            st128(rw, uo, (u32x4){__builtin_amdgcn_perm(ub.x, ua.x, 0x05040100), tag, __builtin_amdgcn_perm(ub.x, ua.x, 0x07060302), tag});
            st128(rw, uo + 16384, (u32x4){__builtin_amdgcn_perm(ub.y, ua.y, 0x05040100), tag, __builtin_amdgcn_perm(ub.y, ua.y, 0x07060302), tag});
            STAMP(1);
            const float dt = rbf(expf(bf2f(ch ? ldt2 >> 16 : ldt2 & 0xffffu)));
            const float a2 = -rbf(expf(bf2f(vlar))) * dt * 1.4426950408889634f;
            const float rev = bf2f(vaim) * dt * 0.15915494309189535f;
            const f32x2 w1 = cexp_at(a2, rev, 1.f), w8 = cexp_at(a2, rev, 8.f), w128 = cexp_at(a2, rev, 128.f);
            const u32 sw = L_SCR + w * 4352 + lane * 4, sr = L_SCR + w * 4352 + c16 * 272 + g4 * 16;
#define PUT(row, z) lds_st<u32>(sw + (row) * 272, pk2((z).x, (z).y))
            u32x4 bf[4];
            {
                f32x2 ze = cmul(w8, w8), zo = w8;
                const f32x2 w16 = ze;
                lds_st<u32>(sw, 0x3f80u);
                PUT(1, zo);
#pragma unroll
                for (int i = 2; i < 16; i += 2) {
                    zo = cmul(zo, w16);
                    PUT(i, ze); PUT(i + 1, zo);
                    if (i < 14) ze = cmul(ze, w16);
                }
#pragma unroll
                for (int k = 0; k < 4; ++k) bf[k] = lds_ld<u32x4>(sr + 64 * k);
            }
            f32x4 acc[3];
#define TILE(t) do { acc[t] = (f32x4){0, 0, 0, 0}; _Pragma("unroll") for (int k = 0; k < 4; ++k) acc[t] = MFMA16(lds_ld<u32x4>(sr + 64 * k), bf[k], acc[t]); } while (0)
            f32x2 e[4];
            {
                f32x2 z = {2.f * (bf2f(vcr) * dt), -2.f * (bf2f(vci) * dt)};
#pragma unroll
                for (int i = 0; i < 8; ++i) {
                    const f32x2 y = cmulc(z, w128);
                    PUT(i, z); PUT(8 + i, y);
                    if (!(i & 1)) e[i >> 1] = y;
                    if (i < 7) z = cmulc(z, w1);
                }
            }
            TILE(0);
#pragma unroll
            for (int s = 0; s < 2; ++s)
#pragma unroll
                for (int i = 0; i < 4; ++i) { e[i] = cmulc(e[i], w128); PUT(4 * s + i, e[i]); }
#pragma unroll
            for (int s = 0; s < 4; ++s) { e[0] = cmulc(e[0], w128); e[2] = cmulc(e[2], w128); PUT(8 + 2 * s, e[0]); PUT(9 + 2 * s, e[2]); }
            TILE(1);
#pragma unroll
            for (int i = 0; i < 9; ++i) { e[0] = cmulc(e[0], w128); PUT(i, e[0]); }
            TILE(2);
#pragma unroll
            for (int t = 0; t < 3; ++t) lds_st<f32x4>(L_SCR + w * 4352 + t * 1024 + lane * 16, acc[t]);
        } else {
            asm volatile("" : "+s"(tag));
            tag = tag * 1664525u + 1013904223u;
        }
    }
    STAMP(2);
    BAR();
    STAMP(3);

    if (w < 4) {
        const u32 mt = w >> 1, nt = w & 1;
        const bool isv = kvwg && nt;
        const u32 xr = 32 * mt + (isv ? ((r32 & 19) | ((r32 & 4) << 1) | ((r32 & 8) >> 1)) : r32);
        const u32 cb = g4 & 1, q = (lane >> 2) & 3, p = lane & 3, ps = (kvwg && cb) ? p ^ (p >> 1) : p;
        u32 xa = L_XS + xr * 128 + ((hh ^ ((xr >> 1) & 7)) << 4);
        u32 wa = L_WS + (8 * hh + q) * 128 + ((64 * nt + 32 * cb + 8 * ps) ^ ((q >> 1) << 6));
        f32x16 acc = {0};
#pragma unroll 1
        for (int a = 0; a < 7; ++a) {
            u32x4 xf[4], wf[4];
#pragma unroll
            for (int b = 0; b < 4; ++b) {
                xf[b] = lds_ld<u32x4>(xa ^ (b << 5));
                wf[b] = cat(lds_tr(wa + b * 2048), lds_tr(wa + b * 2048 + 512));
            }
            if (isv) {
#pragma unroll
                for (int b = 0; b < 4; ++b) acc = MFMA32(xf[b], wf[b], acc);
            } else {
#pragma unroll
                for (int b = 0; b < 4; ++b) acc = MFMA32(wf[b], xf[b], acc);
            }
            xa += 8192; wa += 8192;
            if (a == 1) BAR();
        }
        STAMP(4);
        BAR();
        if (!kvwg) {
            const u32 qo = W_QX + (((2 * rt + mt) * 112 + 16 * cs + 8 * nt) * 32 + lane) * 16;
#pragma unroll
            for (int r = 0; r < 4; ++r)
                st128(rw, qo + r * 1024, (u32x4){pk2(acc[4 * r] * QSCALE, acc[4 * r + 1] * QSCALE), tag, pk2(acc[4 * r + 2] * QSCALE, acc[4 * r + 3] * QSCALE), tag});
        } else {
            u32x4 f0 = {pk2(acc[0], acc[1]), pk2(acc[2], acc[3]), pk2(acc[4], acc[5]), pk2(acc[6], acc[7])};
            u32x4 f1 = {pk2(acc[8], acc[9]), pk2(acc[10], acc[11]), pk2(acc[12], acc[13]), pk2(acc[14], acc[15])};
            u32 slot = lane;
            if (nt) {
                const u32 s = r32 / 14, rem = r32 - 14 * s, h2 = rem / 7, i = rem - 7 * h2;
                slot = 16 * s + 8 * (i >> 2) + 4 * h2 + (i & 3);
                if (r32 >= 28) {
                    slot = 11 + 4 * (r32 & 1) + 8 * (r32 & 2);
                    f0 = f1 = (u32x4){0x3f803f80u, 0x3f803f80u, 0x3f803f80u, 0x3f803f80u};
                }
                slot += 32 * hh;
            } else if (hh) {
                f1.z = 0; f1.w = 0;
            }
            const u32 ko = W_KV + (2 * rt + mt) * 4096 + nt * 2048 + slot * 16;
            st128(rw, ko, f0);
            st128(rw, ko + 1024, f1);
            VMWAIT(0);
            BAR();
        }
        const u32 oo = W_OX + (qt * 32 + 8 * hg + 2 * w) * 1024 + lane * 16;
        st128(rw, oo, (u32x4){~0u, ~0u, ~0u, ~0u});
        st128(rw, oo + 1024, (u32x4){~0u, ~0u, ~0u, ~0u});
        STAMP(5);
        const u32 qi = W_QX + ((qt * 112 + 7 * (4 * hg + w)) * 32 + lane) * 16;
        u32x4 q0, q1, q2, q3;
        for (int spin = 0; spin < SPIN_CAP; ++spin) {
            FENCE();
            q0 = ld128(rw, qi); q1 = ld128(rw, qi + 1024); q2 = ld128(rw, qi + 2048); q3 = ld128(rw, qi + 3072 - 512 * hh);
            const bool ok = q0.y == tag && q0.w == tag && q1.y == tag && q1.w == tag && q2.y == tag && q2.w == tag && q3.y == tag && q3.w == tag;
            if (__all(ok)) break;
        }
        lds_st<u32x4>(L_QS + w * 2048 + lane * 16, (u32x4){q0.x, q0.z, q1.x, q1.z});
        lds_st<u32x4>(L_QS + w * 2048 + 1024 + lane * 16, (u32x4){q2.x, q2.z, hh ? 0u : q3.x, hh ? 0u : q3.z});
    } else {
        if (!kvwg) {
            const u32 t2 = tid - 256;
            const u32 ui = W_UX + cp * 16384 + t2 * 16;
            u32x4 u0 = ld128(rw, ui), u1 = ld128(rw, ui + 8192);
            if (w < 10) {
                const u32 c = (w - 4) / 3, t = (w - 4) - 3 * c;
                const u32 pa = L_SCR + c * 4 * 4352 + t * 1024 + lane * 16;
                f32x4 s = (lds_ld<f32x4>(pa) + lds_ld<f32x4>(pa + 4352)) + (lds_ld<f32x4>(pa + 2 * 4352) + lds_ld<f32x4>(pa + 3 * 4352));
                const u32 dsk2 = *(const CAS u32*)(dsk + 4 * cp);
                if (t == 0 && lane == 0) s.x += bf2f(c ? dsk2 >> 16 : dsk2 & 0xffffu);
                u32 base, s1 = 1, s2 = 2;
                if (t == 0) base = 128 * (g4 >> 1) + 8 * c16 + 4 * (g4 & 1);
                else if (t == 1) { base = 256 + 64 * g4 + (g4 < 2 ? 4 : 2) * c16; s2 = g4 < 2 ? 2 : 32; }
                else { base = 512 + 64 * g4 + c16; s1 = 16; s2 = 32; }
                const u32 ka = L_KC + c * 1552 + 16 + 2 * base, lo = pk2(s.x, s.y), hi = pk2(s.z, s.w);
                lds_st<u16>(ka, lo); lds_st<u16>(ka + 2 * s1, lo >> 16);
                lds_st<u16>(ka + 2 * s2, hi); lds_st<u16>(ka + 2 * (s1 + s2), hi >> 16);
            }
            BAR();
            {
                const u32 c = t2 >> 8, m = t2 & 255;
                const int o = max(0, 131 - 8 * (int)m);
                const float f0 = (float)(8 * (int)m - 3 + o);
                const u32 kb = L_KC + c * 1552 + 16 - 2 * (17024 + o);
                u32 kv[12];
#pragma unroll
                for (int e = 0; e < 12; e += 2) {
                    const u32 b = pk2(f0 + e, f0 + (e + 1));
                    kv[e] = lds_ld<u16>(kb + 2 * (b & 0xffffu));
                    if (e < 10) kv[e + 1] = lds_ld<u16>(kb + 2 * (b >> 16));
                }
                const u32 ta = L_SCR + c * 16640 + (m + 4) * 8;
#pragma unroll
                for (int i = 0; i < 8; ++i) lds_st<u32x2>(ta + i * 2080, (u32x2){kv[i + 3] | (kv[i + 2] << 16), kv[i + 1] | (kv[i] << 16)});
                if (m < 32) lds_st<u32x2>(L_SCR + c * 16640 + (m >> 2) * 2080 + (m & 3) * 8, (u32x2){0, 0});
            }
            for (int spin = 0; spin < SPIN_CAP; ++spin) {
                if (__all(u0.y == tag && u0.w == tag && u1.y == tag && u1.w == tag)) break;
                FENCE();
                u0 = ld128(rw, ui); u1 = ld128(rw, ui + 8192);
            }
            const u32 ua = L_US + ((t2 >> 2) & 3) * 1280 + (16 + (t2 >> 4)) * 16 + (t2 & 3) * 4;
            lds_st<u32>(ua, u0.x); lds_st<u32>(ua + 5120, u0.z);
            lds_st<u32>(ua + 512, u1.x); lds_st<u32>(ua + 5120 + 512, u1.z);
            STAMP(4);
            BAR();
        } else {
            BAR();
            BAR();
            BAR();
            if (tid == 512) st32(rw, W_F1 + rt * 128, tag);
        }
        if (w < 8) {
            if (!kvwg) {
                const u32 c = (w >> 1) & 1, al = w & 1;
                const u32 x0 = 32 + 16 * al + c16 - 8 * g4, x1 = x0 - 4;
                u32 a0 = L_SCR + c * 16640 + (x0 & 7) * 2080 + (x0 >> 3) * 8;
                u32 a1 = L_SCR + c * 16640 + (x1 & 7) * 2080 + (x1 >> 3) * 8;
                u32 ba = L_US + c * 5120 + g4 * 1280 + (16 + c16) * 16;
                f32x4 y[4] = {{0, 0, 0, 0}, {0, 0, 0, 0}, {0, 0, 0, 0}, {0, 0, 0, 0}};
                u32x4 A[4], B[4], An[4], Bn[4];
#define CLOAD(A_, B_, r) _Pragma("unroll") for (int s = 0; s < 4; ++s) { \
                    A_[s] = cat(lds_ld<u32x2>(a0 + 32 * (r) + 512 * s), lds_ld<u32x2>(a1 + 32 * (r) + 512 * s)); B_[s] = lds_ld<u32x4>(ba + 16 - 16 * (r) + 256 * s); }
#define CMMA(A_, B_) _Pragma("unroll") for (int s = 0; s < 4; ++s) _Pragma("unroll") for (int i = s; i < 4; ++i) y[i] = MFMA16(A_[s], B_[i - s], y[i]);
                ba -= 16;
                CLOAD(A, B, 0);
#pragma unroll 1
                for (int r = 0; r < 8; ++r) {
                    CLOAD(An, Bn, 1);
                    CMMA(A, B);
                    a0 += 64; a1 += 64; ba -= 32;
                    CLOAD(A, B, 0);
                    CMMA(An, Bn);
                }
                const u32 d = 2 * cp + c;
                const u32 yo = W_YX + (((d >> 6) * 32 + 2 * c16 + al) * 64 + (d & 63)) * 128 + g4 * 16;
                st128(rw, yo, (u32x4){pk2(y[0].x, y[0].y), pk2(y[0].z, y[0].w), pk2(y[1].x, y[1].y), pk2(y[1].z, y[1].w)});
                st128(rw, yo + 64, (u32x4){pk2(y[2].x, y[2].y), pk2(y[2].z, y[2].w), pk2(y[3].x, y[3].y), pk2(y[3].z, y[3].w)});
            }
        }
    }

    if (w >= 8) {
        const u32 lw = w - 8;
        for (int spin = 0; spin < SPIN_CAP; ++spin) {
            FENCE();
            if (__all(ld32(rw, W_F1 + r32 * 128) == tag)) break;
        }
        STAMP(5);
        const char* kvs = ws + W_KV + lw * 4096 + lane * 16;
        const u32 colb = (c16 ^ (g4 << 2)) << 4;
        const char* wos = wo + (16 * lw + g4) * 896 + 256 * hg + (hg == 3 ? colb & 127 : colb);
#define KVSTAGE(n, slot) _Pragma("unroll") for (int i = 0; i < 4; ++i) dma_dev(kvs + (n) * 16384 + i * 1024, (slot) * 16384 + lw * 4096 + i * 1024);
        KVSTAGE(0, 4); KVSTAGE(1, 5); KVSTAGE(2, 6);
        VMWAIT(8);
        STAMP(6);
        BAR();
        STAMP(9); CSTAMP(14);
        KVSTAGE(3, 7);
        const u32 ycp = 32 * cs + r32, yx = ycp / 28;
        const u32 fw = hh ? xcd + 8 * ((j & ~3u) + (lane & 3)) : (kvwg ? g : yx + 8 * (ycp - 28 * yx + 4));
        u32 f2 = 0, slot = 8;
#pragma unroll 1
        for (u32 it = 0; it < 16; ++it) {
            VMWAIT(8);
            asm volatile("s_barrier" ::: "memory");
            if (it < 12) { KVSTAGE(it + 4, slot); slot = slot == 9 ? 0 : slot + 1; }
            if (it >= 8 && it < 15) {
#pragma unroll
                for (int i = 0; i < 4; ++i) dma(wos + ((it - 8) * 64 + 4 * i) * 896, L_WO + (it - 8) * 16384 + lw * 4096 + i * 1024);
            }
            if (it == 4 && tid == 512) st32(rw, W_F2 + g * 128, tag);
            if (it == 12) asm volatile("global_load_dword %0, %1, off sc1" : "+v"(f2) : "v"(ws + W_F2 + fw * 128) : "memory");
        }
        STAMP(10); CSTAMP(15);
        asm volatile("s_waitcnt vmcnt(0)" : "+v"(f2) :: "memory");
        for (int spin = 0; spin < SPIN_CAP; ++spin) {
            if (__all(f2 == tag)) break;
            FENCE();
            f2 = ld32(rw, W_F2 + fw * 128);
        }
        STAMP(7);
        BAR();
        if (!kvwg) {
            const u32 pos = 2 * lw + hh, yi = W_YX + ((cs * 32 + rt) * 64 + 2 * r32) * 128 + pos * 16;
            const u32x4 ya = ld128(rw, yi), yb = ld128(rw, yi + 128);
            char* yo = out + (16 * rt + 4 * (pos & 3) + 1024 * (pos >> 2)) * 1792 + 896 + 128 * cs + 4 * r32;
#pragma unroll
            for (int r = 0; r < 8; ++r)
                *(u32*)(yo + ((r & 3) + 512 * (r >> 2)) * 1792) = __builtin_amdgcn_perm(yb[r >> 1], ya[r >> 1], (r & 1) ? 0x07060302 : 0x05040100);
        } else if (g == 0 && lw == 0) {
            for (int spin = 0; spin < SPIN_CAP; ++spin) {
                FENCE();
                bool ok = true;
#pragma unroll
                for (int i = 0; i < 4; ++i) ok &= ld32(rw, W_F2 + (lane + 64 * i) * 128) == tag;
                if (__all(ok)) break;
            }
            if (lane == 0) st32(rw, 0, tag);
        }
        STAMP(8);
        BAR();
        BAR();
    } else {
        const u32 hd = w & 3, kh = w >> 2;
        STAMP(6);
        f32x16 o;
        u32x4 qf0, qf1;
        {
            const u32 lb = lane * 16 + kh * 8192, qa = L_QS + hd * 2048 + lane * 16;
#define E4(d, s) "v_exp_f32 v[" #d "+0], v[" #s "+0]\n\tv_exp_f32 v[" #d "+1], v[" #s "+1]\n\tv_exp_f32 v[" #d "+2], v[" #s "+2]\n\tv_exp_f32 v[" #d "+3], v[" #s "+3]\n\t"
#define HALF(S, SN, KO, V1B, V1O, V0B, V0O) \
            "s_waitcnt lgkmcnt(3)\n\tv_mfma_f32_32x32x16_bf16 v[" #SN ":" #SN "+15], v[112:115], v[104:107], 0\n\t" \
            "ds_read_b128 v[112:115], v129 offset:" #KO "\n\t" E4(72, S) \
            "s_waitcnt lgkmcnt(3)\n\tv_mfma_f32_32x32x16_bf16 v[" #SN ":" #SN "+15], v[116:119], v[108:111], v[" #SN ":" #SN "+15]\n\t" \
            "ds_read_b128 v[116:119], v129 offset:" #KO "+1024\n\t" E4(76, S+4) \
            "s_waitcnt lgkmcnt(3)\n\tv_smfmac_f32_32x32x32_bf16 v[88:103], v[124:127], v[80:87], v128\n\t" \
            "ds_read_b128 v[124:127], " #V1B " offset:" #V1O "\n\t" E4(80, S+8) \
            "s_waitcnt lgkmcnt(3)\n\tv_smfmac_f32_32x32x32_bf16 v[88:103], v[120:123], v[72:79], v128\n\t" \
            "ds_read_b128 v[120:123], " #V0B " offset:" #V0O "\n\t" E4(84, S+12)
            u32 sit, snx;
            asm volatile(
                "v_add_u32 v129, 0x10000, %[lb]\n\t"
                "v_mov_b32 v128, 0xdddd\n\t"
                ".irp i,80,81,82,83,84,85,86,87,88,89,90,91,92,93,94,95,96,97,98,99,100,101,102,103\n\tv_mov_b32 v\\i, 0\n\t.endr\n\t"
                "s_mov_b32 %[it], 0\n\ts_mov_b32 %[nx], 0x10000\n\t"
                "s_waitcnt lgkmcnt(0)\n\ts_barrier\n\t"
                "ds_read_b128 v[104:107], %[qa]\n\tds_read_b128 v[112:115], v129\n\t"
                "ds_read_b128 v[108:111], %[qa] offset:1024\n\tds_read_b128 v[116:119], v129 offset:1024\n\t"
                "s_waitcnt lgkmcnt(2)\n\t"
                "v_mfma_f32_32x32x16_bf16 v[40:55], v[112:115], v[104:107], 0\n\t"
                "ds_read_b128 v[112:115], v129 offset:4096\n\t"
                "s_waitcnt lgkmcnt(1)\n\t"
                "v_mfma_f32_32x32x16_bf16 v[40:55], v[116:119], v[108:111], v[40:55]\n\t"
                "ds_read_b128 v[116:119], v129 offset:5120\n\t"
                "ds_read_b128 v[124:127], v129 offset:3072\n\t"
                "ds_read_b128 v[120:123], v129 offset:2048\n\t"
                "1:\n\t"
                "s_add_u32 %[nx], %[nx], 0x4000\n\ts_cmp_eq_u32 %[nx], 0x28000\n\ts_cselect_b32 %[nx], 0, %[nx]\n\t"
                "v_mov_b32 v130, v129\n\tv_add_u32 v129, %[nx], %[lb]\n\t"
                "s_cmp_lg_u32 %[it], 4\n\ts_cbranch_scc1 2f\n\ts_waitcnt vmcnt(0)\n\t2:\n\t"
                "s_barrier\n\t"
                HALF(40, 56, 0, v130, 3072, v130, 6144)
                HALF(56, 40, 4096, v130, 7168, v129, 2048)
                "s_add_u32 %[it], %[it], 1\n\ts_cmp_lg_u32 %[it], 16\n\ts_cbranch_scc1 1b\n\t"
                "s_waitcnt lgkmcnt(0)\n\t"
                "v_smfmac_f32_32x32x32_bf16 v[88:103], v[124:127], v[80:87], v128\n\t"
                "s_nop 12"
                : "={v[88:103]}"(o), "={v[104:107]}"(qf0), "={v[108:111]}"(qf1), [it] "=&s"(sit), [nx] "=&s"(snx)
                : [lb] "{v131}"(lb), [qa] "{v132}"(qa)
                : "memory", "scc", "v40", "v41", "v42", "v43", "v44", "v45", "v46", "v47", "v48", "v49", "v50", "v51", "v52", "v53", "v54", "v55",
                  "v56", "v57", "v58", "v59", "v60", "v61", "v62", "v63", "v64", "v65", "v66", "v67", "v68", "v69", "v70", "v71",
                  "v72", "v73", "v74", "v75", "v76", "v77", "v78", "v79", "v80", "v81", "v82", "v83", "v84", "v85", "v86", "v87",
                  "v112", "v113", "v114", "v115", "v116", "v117", "v118", "v119", "v120", "v121", "v122", "v123", "v124", "v125", "v126", "v127",
                  "v128", "v129", "v130");
        }
        STAMP(7);
        const u32 rsw = L_RS + w * 2048 + lane * 16, rsr = L_RS + (w ^ 4) * 2048 + lane * 16;
#define LO8(v) __builtin_shufflevector(v, v, 0, 1, 2, 3, 4, 5, 6, 7)
#define HI8(v) __builtin_shufflevector(v, v, 8, 9, 10, 11, 12, 13, 14, 15)
#define PAIR_PUT(v) do { keep = kh ? HI8(v) : LO8(v); const f32x8 gv_ = kh ? LO8(v) : HI8(v); \
            lds_st<f32x4>(rsw, __builtin_shufflevector(gv_, gv_, 0, 1, 2, 3)); lds_st<f32x4>(rsw + 1024, __builtin_shufflevector(gv_, gv_, 4, 5, 6, 7)); } while (0)
#define PAIR_GET() do { const f32x4 p0_ = lds_ld<f32x4>(rsr), p1_ = lds_ld<f32x4>(rsr + 1024); keep += __builtin_shufflevector(p0_, p1_, 0, 1, 2, 3, 4, 5, 6, 7); } while (0)
        f32x8 keep;
        PAIR_PUT(o);
        BAR();
        PAIR_GET();
        if (__builtin_expect(!__all(keep[7] > 0x1p-30f && keep[7] < 0x1p64f), 0)) {
            const f32x16 zero = {0};
            float m = -__builtin_inff();
#pragma unroll 1
            for (u32 t = 0; t < 64; ++t) {
                const u32 ko = W_KV + t * 4096 + lane * 16;
                const f32x16 s = MFMA32(ld128(rw, ko + 1024), qf1, MFMA32(ld128(rw, ko), qf0, zero));
#pragma unroll
                for (int r = 0; r < 16; ++r) m = fmaxf(m, s[r]);
            }
            m = fmaxf(m, __shfl_xor(m, 32));
            o = zero;
#pragma unroll 1
            for (u32 t = 0; t < 64; ++t) {
                const u32 ko = W_KV + t * 4096 + lane * 16;
                f32x16 s = MFMA32(ld128(rw, ko + 1024), qf1, MFMA32(ld128(rw, ko), qf0, zero));
#pragma unroll
                for (int r = 0; r < 16; ++r) s[r] = __builtin_amdgcn_exp2f(s[r] - m);
                o = __builtin_amdgcn_smfmac_f32_32x32x32_bf16(__builtin_bit_cast(bf16x8, ld128(rw, ko + 2048)), __builtin_bit_cast(bf16x16, LO8(s)), o, 0xdddd, 0, 0);
                o = __builtin_amdgcn_smfmac_f32_32x32x32_bf16(__builtin_bit_cast(bf16x8, ld128(rw, ko + 3072)), __builtin_bit_cast(bf16x16, HI8(s)), o, 0xdddd, 0, 0);
            }
            keep = kh ? HI8(o) : LO8(o);
        }
        {
            const float inv = __builtin_amdgcn_rcpf(keep[7]);
            const u32x4 f = {min(pk2(keep[0] * inv, keep[1] * inv), ~1u), min(pk2(keep[2] * inv, keep[3] * inv), ~1u),
                             min(pk2(keep[4] * inv, keep[5] * inv), ~1u), pk2(keep[6] * inv, 0.f)};
            const u32 ks = 8 * hg + 2 * hd + kh;
            st128(rw, W_OX + (qt * 32 + ks) * 1024 + lane * 16, f);
            lds_st<u32x4>(L_OS + ks * 1024 + lane * 16, f);
        }
        STAMP(8);
        const u32 k1 = ((8 * hg + 8 + w) & 31) * 1024 + lane * 16, k2 = ((8 * hg + 16 + w) & 31) * 1024 + lane * 16, k3 = ((8 * hg + 24 + w) & 31) * 1024 + lane * 16;
        const u32 oi = W_OX + qt * 32768;
        u32x4 wf[16];
        {
            const u32 cb = g4 & 1, q = (lane >> 2) & 3, p = lane & 3;
            const u32 col = 64 * hd + 32 * cb + 8 * (((p & 1) << 1) | (p >> 1));
            const u32 ra = 224 * kh + 7 * hh + q, rb = 224 * kh + 7 * hh + min(4 + q, 6u);
            const u32 wa = L_WO + ra * 256 + (col ^ ((ra & 3) << 6)), wb = L_WO + rb * 256 + (col ^ ((rb & 3) << 6));
#pragma unroll
            for (int i = 0; i < 16; ++i)
                wf[i] = cat(lds_tr((wa ^ ((i & 1) << 7)) + i * 3584), lds_tr((wb ^ ((i & 1) << 7)) + i * 3584));
        }
        u32x4 o1, o2, o3;
        for (int spin = 0; spin < SPIN_CAP; ++spin) {
            FENCE();
            o1 = ld128(rw, oi + k1); o2 = ld128(rw, oi + k2); o3 = ld128(rw, oi + k3);
            const u32 mx = max(max(max(max(o1.x, o1.y), max(o1.z, o1.w)), max(max(o2.x, o2.y), max(o2.z, o2.w))), max(max(o3.x, o3.y), max(o3.z, o3.w)));
            if (__all(mx != ~0u)) break;
        }
        lds_st<u32x4>(L_OS + k1, o1); lds_st<u32x4>(L_OS + k2, o2); lds_st<u32x4>(L_OS + k3, o3);
        STAMP(9);
        BAR();
        f32x16 acc = {0};
#pragma unroll
        for (int i = 0; i < 16; ++i) acc = MFMA32(wf[i], lds_ld<u32x4>(L_OS + (16 * kh + i) * 1024 + lane * 16), acc);
        PAIR_PUT(acc);
        STAMP(10);
        BAR();
        PAIR_GET();
        if (hg < 3 || hd < 2)
            *(u32x4*)(out + (32 * qt + r32) * 1792 + 256 * hg + 64 * hd + 32 * kh + 16 * hh) =
                (u32x4){pk2(keep[0], keep[1]), pk2(keep[2], keep[3]), pk2(keep[4], keep[5]), pk2(keep[6], keep[7])};
    }
    STAMP(11);
    VMWAIT(0);
    STAMP(12);
    if (lane < 16) *(u32*)(ws + (32u << 20) + ((g * 12 + w) * 16 + lane) * 4) = stamp_;
}

__global__ void k_amp(const u32* st, u32 mul) {
    __shared__ u32 x0[8], mx, mn;
    const int g = threadIdx.x;
    u64 now; asm volatile("s_memrealtime %0\n s_waitcnt lgkmcnt(0)" : "=s"(now) :: "memory");
    if (g < 8) x0[g] = ~0u;
    if (g == 0) { mx = 0; }
    __syncthreads();
    u32 a = ~0u, b = 0;
    for (int w = 0; w < 12; ++w) { a = min(a, st[(g * 12 + w) * 16]); b = max(b, st[(g * 12 + w) * 16 + (STAT < 16 ? STAT : 12)]); }
    atomicMin(&x0[g & 7], a); atomicMax(&mx, b);
    __syncthreads();
    u32 last = 0, first = ~0u;
    for (int i = 0; i < 8; ++i) { last = max(last, x0[i]); first = min(first, x0[i]); }
    u32 v = STAT == 20 ? last - first : mx - last;
    if (STAT == 23) v = (u32)now - mx;
    if (STAT == 24) v = last - st[1 << 18];
    if (STAT == 22) { const u32* q = st + (100 * 12 + 8) * 16; v = (q[15] - q[14]) * 10 / (q[10] - q[9]); }
    if (g < 64) { u64 d = (u64)min(v, 10000u) * mul, s, t;
        asm volatile("s_memrealtime %0\n s_waitcnt lgkmcnt(0)" : "=s"(s) :: "memory");
        do { asm volatile("s_memrealtime %0\n s_waitcnt lgkmcnt(0)" : "=s"(t) :: "memory"); } while (t - s < d); }
}
__global__ void k_pre(u32* st) { u64 now; asm volatile("s_memrealtime %0\n s_waitcnt lgkmcnt(0)" : "=s"(now) :: "memory"); if (threadIdx.x == 0) st[1 << 18] = (u32)now; }

extern "C" void kernel_launch(void* const* d_in, const int* in_sizes, int n_in, void* d_out, int out_size, void* d_ws, size_t ws_size,
                              hipStream_t stream) {
    fused_kernel<<<256, 768, 0, stream>>>((const char*)d_in[0], (const char*)d_in[1], (const char*)d_in[2], (const char*)d_in[3],
                                          (const char*)d_in[4], (const char*)d_in[5], (const u16*)d_in[6], (const u16*)d_in[7],
                                          (const u16*)d_in[8], (const u16*)d_in[9], (const char*)d_in[10], (const char*)d_in[11],
                                          (char*)d_out, (char*)d_ws);
    k_amp<<<1, 256, 0, stream>>>((const u32*)((char*)d_ws + (32u << 20)), 0u);
}
```

```cpp
#include <hip/hip_runtime.h>
#include <stdint.h>
typedef unsigned u32;
typedef unsigned long long u64;

__global__ void __launch_bounds__(512, 2) fused_kernel(
    void* ws, const void* x, const void* wq, const void* wk, const void* wv, void* out, const void* u, const void* lar,
    const void* aim, const void* cre, const void* cim, const void* ldt, const void* dsk, const void* wo) {
#if defined(__HIP_DEVICE_COMPILE__)
    __shared__ __attribute__((aligned(1024))) char lds[163840];
    asm volatile("" ::"s"((u32)(uintptr_t)lds));
    asm volatile(
    "s_load_dwordx16 s[4:19], s[0:1], 0x8\n"
    "s_load_dwordx2 s[52:53], s[0:1], 0x0\n"
    "s_load_dwordx8 s[20:27], s[0:1], 0x48\n"
    "s_load_dwordx2 s[28:29], s[0:1], 0x68\n"
    "s_getpc_b64 s[58:59]\n"
    ".L_PC:\n"
    "s_bfe_u32 s62, s2, 196611\n"
    "v_and_b32 v7, 496, v0\n"
    "s_lshl_b32 s62, s62, 12\n"
    "v_lshl_add_u32 v7, v7, 3, s62\n"
    "v_min_u32 v7, .L_LAST-.L_PC, v7\n"
    "global_load_dword v58, v7, s[58:59]\n"
    "v_readfirstlane_b32 s32, v0\n"
    "v_and_b32 v1, 63, v0\n"
    "s_and_b32 s33, s2, 7\n"
    "s_lshr_b32 s34, s2, 3\n"
    "s_lshr_b32 s32, s32, 6\n"
    "s_and_b32 s39, s34, 3\n"
    "s_lshr_b32 s42, s34, 2\n"
    "s_lshl2_add_u32 s35, s33, s39\n"
    "s_sub_u32 s36, s42, 1\n"
    "s_lshl1_add_u32 s41, s39, s42\n"
    "s_add_u32 s41, s41, s33\n"
    "s_mul_i32 s43, s41, 37\n"
    "s_lshr_b32 s43, s43, 8\n"
    "s_mul_i32 s43, s43, 7\n"
    "s_sub_u32 s41, s41, s43\n"
    "s_lshl_b32 s41, s41, 13\n"
    "s_and_b32 s43, s32, 3\n"
    "v_lshrrev_b32 v6, 4, v1\n"
    "s_cmp_lt_u32 s32, 4\n"
    "s_cbranch_scc1 .L_A\n"
    "s_lshl3_add_u32 s38, s33, s42\n"
    "v_lshlrev_b32 v2, 4, v1\n"
    "v_and_b32 v3, 31, v0\n"
    "v_lshrrev_b32 v4, 5, v1\n"
    "v_and_b32 v5, 15, v0\n"
    "s_mov_b32 s54, 7340032\n"
    "s_mov_b32 s55, 0x20000\n"
    "s_lshl2_add_u32 s58, s43, 161312\n"
    "v_mov_b32 v250, s58\n"
    "v_and_b32 v251, 3, v0\n"
    "v_lshlrev_b32 v251, 2, v251\n"
    "v_add_u32 v251, 161312, v251\n"
    "s_lshr_b32 s63, s43, 1\n"
    "s_and_b32 s64, s43, 1\n"
    "s_mul_i32 s37, s33, 28\n"
    "s_max_u32 s58, s34, 4\n"
    "s_add_u32 s37, s37, s58\n"
    "s_sub_u32 s37, s37, 4\n"
    "s_lshl_b32 s58, s37, 10\n"
    "v_lshl_add_u32 v51, v0, 1, s58\n"
    "v_mov_b32 v52, 0\n"
    "v_mov_b32 v54, 0\n"
    "v_mov_b64 v[56:57], 0\n"
    "s_mul_i32 s58, s32, 4352\n"
    "v_mul_u32_u24 v33, 272, v5\n"
    "v_lshl_add_u32 v33, v6, 4, v33\n"
    "v_add_u32 v33, s58, v33\n"
    "v_add_u32 v34, s58, v2\n"
    "s_mov_b32 s90, 0x3fb8aa3b\n"
    "s_mov_b32 s92, 0x32a57060\n"
    "s_mov_b32 s94, 0x3f317218\n"
    "v_bfe_u32 v7, v0, 3, 5\n"
    "v_lshrrev_b32 v8, 4, v0\n"
    "v_xor_b32 v8, v8, v0\n"
    "v_and_b32 v8, 7, v8\n"
    "s_mul_i32 s58, s35, 57344\n"
    "v_mul_u32_u24 v9, 896, v7\n"
    "v_lshl_add_u32 v28, v8, 4, v9\n"
    "v_add_u32 v28, s58, v28\n"
    "v_add_u32 v29, 28672, v28\n"
    "v_lshlrev_b32 v8, 2, v6\n"
    "v_xor_b32 v8, v8, v0\n"
    "v_and_b32 v8, 7, v8\n"
    "s_lshl_b32 s56, s43, 10\n"
    "s_mov_b32 s46, 1835008\n"
    "s_mov_b32 s47, 0x20000\n"
    "s_mov_b32 s69, 0\n"
    "s_mov_b32 s65, 0\n"
    "v_mov_b32 v31, 0\n"
    "s_cmp_lg_u32 s42, 0\n"
    "s_cbranch_scc1 .L_HQ\n"
    "v_and_b32 v9, 3, v8\n"
    "v_lshlrev_b32 v9, 4, v9\n"
    "v_min_u32 v9, 40, v9\n"
    "v_mad_u32_u24 v30, v7, 56, v9\n"
    "v_cmp_lt_u32 vcc, 3, v8\n"
    "s_mov_b32 s66, 28\n"
    "s_waitcnt lgkmcnt(0)\n"
    "v_mov_b32 v24, s8\n"
    "v_mov_b32 v25, s9\n"
    "v_mov_b32 v26, s10\n"
    "v_mov_b32 v27, s11\n"
    "v_cndmask_b32 v24, v24, v26, vcc\n"
    "v_cndmask_b32 v25, v25, v27, vcc\n"
    "v_lshl_add_u64 v[30:31], v[30:31], 0, v[24:25]\n"
    "s_branch .L_HL\n"
    ".L_HQ:\n"
    "s_lshl_b32 s58, s36, 7\n"
    "v_lshl_add_u32 v30, v8, 4, v9\n"
    "v_add_u32 v30, s58, v30\n"
    "s_mov_b32 s66, 448\n"
    "s_waitcnt lgkmcnt(0)\n"
    "v_lshl_add_u64 v[30:31], v[30:31], 0, s[6:7]\n"
    ".L_HL:\n"
    "s_mov_b64 s[44:45], s[4:5]\n"
    "s_lshl_b32 s67, s66, 6\n"
    ".L_HLL:\n"
    "s_add_u32 m0, s41, s56\n"
    "s_lshr_b32 s57, s41, 6\n"
    "buffer_load_dwordx4 v28, s[44:47], s57 offen lds\n"
    "s_mul_i32 s68, s57, s66\n"
    "v_lshl_add_u64 v[26:27], v[30:31], 0, s[68:69]\n"
    "s_add_u32 m0, m0, 57344\n"
    "s_add_u32 s68, s68, s67\n"
    "global_load_lds_dwordx4 v[26:27], off\n"
    "s_sub_u32 m0, m0, 53248\n"
    "v_lshl_add_u64 v[26:27], v[30:31], 0, s[68:69]\n"
    "buffer_load_dwordx4 v29, s[44:47], s57 offen lds\n"
    "s_add_u32 m0, m0, 57344\n"
    "s_add_u32 s41, s41, 8192\n"
    "global_load_lds_dwordx4 v[26:27], off\n"
    "s_cmp_eq_u32 s41, 57344\n"
    "s_cselect_b32 s41, 0, s41\n"
    "s_add_u32 s65, s65, 1\n"
    "s_cmp_eq_u32 s65, 2\n"
    "s_cbranch_scc1 .L_HPRE\n"
    ".L_HPB:\n"
    "s_cmp_le_u32 s65, 3\n"
    "s_cbranch_scc1 .L_HLL\n"
    "s_waitcnt vmcnt(12)\n"
    "s_barrier\n"
    "s_cmp_lg_u32 s65, 7\n"
    "s_cbranch_scc1 .L_HLL\n"
    "s_cmp_eq_u32 s42, 0\n"
    "s_cbranch_scc1 .L_HWL\n"
    "global_load_short_d16_hi v52, v51, s[16:17]\n"
    "global_load_short_d16_hi v54, v51, s[18:19]\n"
    "global_load_short_d16_hi v56, v51, s[20:21]\n"
    "global_load_short_d16_hi v57, v51, s[22:23]\n"
    "s_branch .L_HT\n"
    ".L_A:\n"
    "s_lshl3_add_u32 s38, s33, s42\n"
    "v_lshlrev_b32 v2, 4, v1\n"
    "v_and_b32 v3, 31, v0\n"
    "v_lshrrev_b32 v4, 5, v1\n"
    "v_and_b32 v5, 15, v0\n"
    "s_mov_b32 s54, 7340032\n"
    "s_mov_b32 s55, 0x20000\n"
    "s_lshl2_add_u32 s58, s43, 161312\n"
    "v_mov_b32 v250, s58\n"
    "v_and_b32 v251, 3, v0\n"
    "v_lshlrev_b32 v251, 2, v251\n"
    "v_add_u32 v251, 161312, v251\n"
    "s_lshr_b32 s63, s43, 1\n"
    "s_and_b32 s64, s43, 1\n"
    "s_mul_i32 s37, s33, 28\n"
    "s_max_u32 s58, s34, 4\n"
    "s_add_u32 s37, s37, s58\n"
    "s_sub_u32 s37, s37, 4\n"
    "v_mov_b32 v16, 0\n"
    "v_lshlrev_b32 v59, 2, v1\n"
    "v_add_u32 v7, 161312, v59\n"
    "ds_write2st64_b32 v7, v16, v16 offset0:0 offset1:1\n"
    "v_and_b32 v8, 3, v0\n"
    "s_cmp_eq_u32 s42, 0\n"
    "s_cselect_b32 s58, 1, 0\n"
    "s_cselect_b32 s59, s64, 0\n"
    "s_mul_i32 s59, s59, 3\n"
    "v_bfe_u32 v9, v0, 1, 1\n"
    "v_and_b32 v9, v9, v6\n"
    "v_and_b32 v9, s58, v9\n"
    "v_xor_b32 v8, v8, v9\n"
    "v_lshrrev_b32 v9, 1, v0\n"
    "v_xor_b32 v9, v9, v0\n"
    "v_and_b32 v9, 4, v9\n"
    "v_mul_u32_u24 v9, s59, v9\n"
    "v_xor_b32 v7, v3, v9\n"
    "v_lshrrev_b32 v9, 1, v7\n"
    "v_xor_b32 v9, v9, v4\n"
    "v_and_b32 v9, 7, v9\n"
    "v_lshlrev_b32 v9, 4, v9\n"
    "v_lshl_add_u32 v44, v7, 7, v9\n"
    "s_lshl_b32 s58, s63, 12\n"
    "v_add_u32 v44, s58, v44\n"
    "v_bfe_u32 v9, v0, 2, 2\n"
    "v_lshl_add_u32 v7, v4, 3, v9\n"
    "v_and_b32 v45, 16, v0\n"
    "v_lshlrev_b32 v45, 1, v45\n"
    "v_lshl_add_u32 v45, v8, 3, v45\n"
    "v_lshrrev_b32 v9, 1, v9\n"
    "v_xor_b32 v9, s64, v9\n"
    "v_lshl_add_u32 v45, v9, 6, v45\n"
    "v_lshl_add_u32 v45, v7, 7, v45\n"
    "v_add_u32 v45, 57344, v45\n"
    "s_mov_b32 s65, 0\n"
    "v_mov_b64 v[224:225], 0\n"
    "v_mov_b64 v[144:145], 0\n"
    "v_mov_b64 v[226:227], 0\n"
    "v_mov_b64 v[146:147], 0\n"
    "v_mov_b64 v[228:229], 0\n"
    "v_mov_b64 v[148:149], 0\n"
    "v_mov_b64 v[230:231], 0\n"
    "v_mov_b64 v[150:151], 0\n"
    "v_mov_b64 v[232:233], 0\n"
    "v_mov_b64 v[152:153], 0\n"
    "v_mov_b64 v[234:235], 0\n"
    "v_mov_b64 v[154:155], 0\n"
    "v_mov_b64 v[236:237], 0\n"
    "v_mov_b64 v[156:157], 0\n"
    "v_mov_b64 v[238:239], 0\n"
    "v_mov_b64 v[158:159], 0\n"
    "s_lshl_b32 s58, s37, 10\n"
    "v_lshl_add_u32 v51, v0, 1, s58\n"
    "v_mov_b32 v52, 0\n"
    "v_mov_b32 v54, 0\n"
    "v_mov_b64 v[56:57], 0\n"
    "s_mul_i32 s58, s32, 4352\n"
    "v_mul_u32_u24 v33, 272, v5\n"
    "v_lshl_add_u32 v33, v6, 4, v33\n"
    "v_add_u32 v33, s58, v33\n"
    "v_add_u32 v34, s58, v2\n"
    "s_mov_b32 s90, 0x3fb8aa3b\n"
    "s_mov_b32 s92, 0x32a57060\n"
    "s_mov_b32 s94, 0x3f317218\n"
    "s_lshl_b32 s62, s37, 2\n"
    "s_waitcnt lgkmcnt(0)\n"
    "s_load_dword s3, s[24:25], s62\n"
    "s_load_dword s61, s[26:27], s62\n"
    "s_load_dword s40, s[52:53], 0x0\n"
    "v_and_b32 v9, 3, v0\n"
    "v_bfe_u32 v13, v0, 2, 1\n"
    "v_lshl_add_u32 v13, v9, 3, v13\n"
    "s_lshl_b32 s59, s32, 1\n"
    "v_add_u32 v13, s59, v13\n"
    "s_mul_i32 s62, s39, 7\n"
    "s_lshr_b32 s59, s62, 2\n"
    "s_add_u32 s62, s62, 6\n"
    "s_lshr_b32 s62, s62, 2\n"
    "v_add_u32 v9, s59, v9\n"
    "v_min_u32 v9, s62, v9\n"
    "s_lshr_b32 s59, s42, 1\n"
    "s_lshl3_add_u32 s59, s59, s33\n"
    "s_add_u32 s59, s59, 32\n"
    "v_lshl_add_u32 v9, v9, 5, s59\n"
    "v_cmp_gt_u32 vcc, 8, v1\n"
    "s_lshl2_add_u32 s59, s38, s39\n"
    "s_mul_i32 s59, s59, 7168\n"
    "v_cndmask_b32 v13, v9, v13, vcc\n"
    "v_lshlrev_b32 v13, 7, v13\n"
    "v_add_u32 v13, 4096, v13\n"
    "s_add_u32 s59, s59, 1048576\n"
    "s_lshl_b32 s68, s32, 10\n"
    "s_add_u32 s69, s59, s68\n"
    "s_add_u32 s71, s69, 3072\n"
    "s_add_u32 s68, s68, 40960\n"
    "s_add_u32 s70, s68, 3072\n"
    "s_lshl_b32 s59, s32, 16\n"
    "s_add_u32 s59, s59, 131072\n"
    "v_add_u32 v11, s59, v2\n"
    "s_lshl_b32 s58, s34, 11\n"
    "s_add_u32 s59, s59, s58\n"
    "v_lshl_add_u32 v14, v5, 7, s59\n"
    "s_mov_b32 s73, 4096\n"
    "s_mov_b32 s74, 8192\n"
    "s_mov_b32 s75, 12288\n"
    "v_mov_b64 v[72:73], 0\n"
    "v_mov_b64 v[74:75], 0\n"
    "v_lshl_add_u32 v7, s64, 2, v6\n"
    "v_mul_u32_u24 v7, 1280, v7\n"
    "v_lshl_add_u32 v7, v5, 4, v7\n"
    "v_add_u32 v7, 147968, v7\n"
    "ds_write_b128 v7, v[72:75]\n"
    "v_mul_u32_u24 v7, 1552, v4\n"
    "v_add_u32 v7, 158208, v7\n"
    "ds_write_b128 v7, v[72:75]\n"
    "v_lshrrev_b32 v7, 2, v1\n"
    "v_mul_u32_u24 v7, 2080, v7\n"
    "v_and_b32 v8, 3, v0\n"
    "v_lshl_add_u32 v7, v8, 3, v7\n"
    "v_add_u32 v7, 114688, v7\n"
    "ds_write_b64 v7, v[72:73]\n"
    "v_and_b32 v7, 16, v0\n"
    "v_lshrrev_b32 v7, 1, v7\n"
    "v_lshl_add_u32 v7, v5, 4, v7\n"
    "v_lshl_add_u32 v7, v4, 8, v7\n"
    "s_mul_i32 s59, s32, 28672\n"
    "v_add_u32 v12, s59, v2\n"
    "v_mov_b32 v10, 0xdddd\n"
    "s_add_u32 s59, s32, 1\n"
    "s_and_b32 s59, s59, 3\n"
    "s_mul_i32 s59, s59, 1792\n"
    "v_add_u32 v28, s59, v7\n"
    "s_add_u32 s59, s32, 2\n"
    "s_and_b32 s59, s59, 3\n"
    "s_mul_i32 s59, s59, 1792\n"
    "v_add_u32 v29, s59, v7\n"
    "s_add_u32 s59, s32, 3\n"
    "s_and_b32 s59, s59, 3\n"
    "s_mul_i32 s59, s59, 1792\n"
    "v_add_u32 v30, s59, v7\n"
    "s_add_u32 s59, s32, 4\n"
    "s_and_b32 s59, s59, 3\n"
    "s_mul_i32 s59, s59, 1792\n"
    "v_add_u32 v31, s59, v7\n"
    "s_sub_u32 s59, s32, 1\n"
    "s_and_b32 s59, s59, 3\n"
    "s_mul_i32 s59, s59, 28672\n"
    "s_add_u32 s59, s59, 20480\n"
    "v_add_u32 v241, s59, v2\n"
    "s_sub_u32 s59, s32, 2\n"
    "s_and_b32 s59, s59, 3\n"
    "s_mul_i32 s59, s59, 28672\n"
    "s_add_u32 s59, s59, 24576\n"
    "v_add_u32 v242, s59, v2\n"
    "s_sub_u32 s59, s32, 3\n"
    "s_and_b32 s59, s59, 3\n"
    "s_mul_i32 s59, s59, 28672\n"
    "s_add_u32 s59, s59, 0\n"
    "v_add_u32 v243, s59, v2\n"
    "s_add_u32 s58, s39, 0\n"
    "s_and_b32 s58, s58, 3\n"
    "s_mul_i32 s80, s58, 20480\n"
    "s_lshl2_add_u32 s58, s58, s32\n"
    "s_lshl_b32 s58, s58, 11\n"
    "v_add_u32 v244, s58, v2\n"
    "s_add_u32 s58, s39, 1\n"
    "s_and_b32 s58, s58, 3\n"
    "s_mul_i32 s81, s58, 20480\n"
    "s_lshl2_add_u32 s58, s58, s32\n"
    "s_lshl_b32 s58, s58, 11\n"
    "v_add_u32 v245, s58, v2\n"
    "s_add_u32 s58, s39, 2\n"
    "s_and_b32 s58, s58, 3\n"
    "s_mul_i32 s82, s58, 20480\n"
    "s_lshl2_add_u32 s58, s58, s32\n"
    "s_lshl_b32 s58, s58, 11\n"
    "v_add_u32 v246, s58, v2\n"
    "s_add_u32 s58, s39, 3\n"
    "s_and_b32 s58, s58, 3\n"
    "s_mul_i32 s83, s58, 20480\n"
    "s_lshl2_add_u32 s58, s58, s32\n"
    "s_lshl_b32 s58, s58, 11\n"
    "v_add_u32 v247, s58, v2\n"
    "s_lshl_b32 s88, s38, 15\n"
    "s_add_u32 s88, s88, 5242880\n"
    "v_mov_b64 v[64:65], 0\n"
    "v_mov_b64 v[66:67], 0\n"
    "v_mov_b64 v[68:69], 0\n"
    "v_mov_b64 v[70:71], 0\n"
    "v_mov_b64 v[72:73], 0\n"
    "v_mov_b64 v[74:75], 0\n"
    "v_mov_b64 v[76:77], 0\n"
    "v_mov_b64 v[78:79], 0\n"
    "v_mov_b64 v[80:81], 0\n"
    "v_mov_b64 v[82:83], 0\n"
    "v_mov_b64 v[84:85], 0\n"
    "v_mov_b64 v[86:87], 0\n"
    "v_mov_b64 v[88:89], 0\n"
    "v_mov_b64 v[90:91], 0\n"
    "v_mov_b64 v[92:93], 0\n"
    "v_mov_b64 v[94:95], 0\n"
    "v_mov_b64 v[96:97], 0\n"
    "v_mov_b64 v[98:99], 0\n"
    "v_mov_b64 v[100:101], 0\n"
    "v_mov_b64 v[102:103], 0\n"
    "v_mov_b64 v[104:105], 0\n"
    "v_mov_b64 v[106:107], 0\n"
    "v_mov_b64 v[108:109], 0\n"
    "v_mov_b64 v[110:111], 0\n"
    "v_mov_b64 v[112:113], 0\n"
    "v_mov_b64 v[114:115], 0\n"
    "v_mov_b64 v[116:117], 0\n"
    "v_mov_b64 v[118:119], 0\n"
    "v_mov_b64 v[120:121], 0\n"
    "v_mov_b64 v[122:123], 0\n"
    "v_mov_b64 v[124:125], 0\n"
    "v_mov_b64 v[126:127], 0\n"
    "s_setprio 3\n"
    "s_cmp_eq_u32 s42, 0\n"
    "s_cbranch_scc1 .L_KVA\n"
    "s_lshl1_add_u32 s62, s35, s63\n"
    "s_mul_i32 s62, s62, 14\n"
    "s_lshl1_add_u32 s62, s36, s62\n"
    "s_add_u32 s62, s62, s64\n"
    "s_lshl_b32 s62, s62, 11\n"
    "s_add_u32 s62, s62, 1048576\n"
    "s_mov_b32 s66, 0x3e8b97f6\n"
    "v_and_b32 v240, 16, v0\n"
    "v_lshlrev_b32 v240, 5, v240\n"
    "v_lshl_add_u32 v240, v4, 8, v240\n"
    "v_lshl_add_u32 v240, v5, 4, v240\n"
    "s_waitcnt lgkmcnt(0)\n"
    "s_mul_i32 s40, s40, 0x19660d\n"
    "s_add_u32 s40, s40, 0x3c6ef35f\n"
    "s_lshl_b32 s3, s3, 16\n"
    "v_mov_b32 v53, s3\n"
    "s_mov_b32 s67, 5\n"
    ".L_GQ:\n"
    "v_add_u32 v46, s41, v44\n"
    "v_add_u32 v50, s41, v45\n"
    "v_xor_b32 v47, 32, v46\n"
    "v_xor_b32 v48, 64, v46\n"
    "v_xor_b32 v49, 0x60, v46\n"
    "s_add_u32 s41, s41, 8192\n"
    "s_cmp_eq_u32 s41, 57344\n"
    "s_cselect_b32 s41, 0, s41\n"
    "s_add_u32 s65, s65, 1\n"
    "s_cmp_lg_u32 s65, s67\n"
    "s_cbranch_scc1 .L_GQN\n"
    "global_load_short_d16_hi v52, v51, s[16:17]\n"
    "global_load_short_d16_hi v54, v51, s[18:19]\n"
    "global_load_short_d16_hi v56, v51, s[20:21]\n"
    "global_load_short_d16_hi v57, v51, s[22:23]\n"
    ".L_GQN:\n"
    "s_cmp_lg_u32 s65, 7\n"
    "s_barrier\n"
    "ds_read_b128 v[128:131], v46\n"
    "ds_read_b64_tr_b16 v[132:133], v50 offset:0\n"
    "ds_read_b64_tr_b16 v[134:135], v50 offset:512\n"
    "s_waitcnt lgkmcnt(6)\n"
    "v_mfma_f32_32x32x16_bf16 v[224:239], v[148:151], v[144:147], v[224:239]\n"
    "ds_read_b128 v[136:139], v47\n"
    "ds_read_b64_tr_b16 v[140:141], v50 offset:2048\n"
    "ds_read_b64_tr_b16 v[142:143], v50 offset:2560\n"
    "ds_read_b128 v[144:147], v48\n"
    "ds_read_b64_tr_b16 v[148:149], v50 offset:4096\n"
    "ds_read_b64_tr_b16 v[150:151], v50 offset:4608\n"
    "s_waitcnt lgkmcnt(9)\n"
    "v_mfma_f32_32x32x16_bf16 v[224:239], v[156:159], v[152:155], v[224:239]\n"
    "ds_read_b128 v[152:155], v49\n"
    "ds_read_b64_tr_b16 v[156:157], v50 offset:6144\n"
    "ds_read_b64_tr_b16 v[158:159], v50 offset:6656\n"
    "s_waitcnt lgkmcnt(9)\n"
    "v_mfma_f32_32x32x16_bf16 v[224:239], v[132:135], v[128:131], v[224:239]\n"
    "s_waitcnt lgkmcnt(6)\n"
    "v_mfma_f32_32x32x16_bf16 v[224:239], v[140:143], v[136:139], v[224:239]\n"
    "s_cbranch_scc1 .L_GQ\n"
    "s_waitcnt lgkmcnt(3)\n"
    "v_mfma_f32_32x32x16_bf16 v[224:239], v[148:151], v[144:147], v[224:239]\n"
    "s_waitcnt lgkmcnt(0)\n"
    "v_mfma_f32_32x32x16_bf16 v[224:239], v[156:159], v[152:155], v[224:239]\n"
    "s_barrier\n"
    "ds_write_b32 v59, v16 offset:48128\n"
    "s_cmp_eq_u32 s42, 0\n"
    "s_cbranch_scc1 .L_EK\n"
    "s_nop 7\n"
    "v_pk_mul_f32 v[224:225], v[224:225], s[66:67] op_sel_hi:[1,0]\n"
    "v_pk_mul_f32 v[226:227], v[226:227], s[66:67] op_sel_hi:[1,0]\n"
    "v_pk_mul_f32 v[228:229], v[228:229], s[66:67] op_sel_hi:[1,0]\n"
    "v_pk_mul_f32 v[230:231], v[230:231], s[66:67] op_sel_hi:[1,0]\n"
    "v_cvt_pk_bf16_f32 v16, v224, v225\n"
    "v_cvt_pk_bf16_f32 v18, v228, v229\n"
    "v_cvt_pk_bf16_f32 v17, v226, v227\n"
    "v_cvt_pk_bf16_f32 v19, v230, v231\n"
    "v_permlane16_swap_b32 v16, v18\n"
    "s_nop 0\n"
    "v_permlane16_swap_b32 v17, v19\n"
    "buffer_store_dwordx4 v[16:19], v240, s[52:55], s62 offen offset:0 sc1\n"
    "v_pk_mul_f32 v[232:233], v[232:233], s[66:67] op_sel_hi:[1,0]\n"
    "v_pk_mul_f32 v[234:235], v[234:235], s[66:67] op_sel_hi:[1,0]\n"
    "v_pk_mul_f32 v[236:237], v[236:237], s[66:67] op_sel_hi:[1,0]\n"
    "v_pk_mul_f32 v[238:239], v[238:239], s[66:67] op_sel_hi:[1,0]\n"
    "v_cvt_pk_bf16_f32 v20, v232, v233\n"
    "v_cvt_pk_bf16_f32 v22, v236, v237\n"
    "v_cvt_pk_bf16_f32 v21, v234, v235\n"
    "v_cvt_pk_bf16_f32 v23, v238, v239\n"
    "v_permlane16_swap_b32 v20, v22\n"
    "s_nop 0\n"
    "v_permlane16_swap_b32 v21, v23\n"
    "buffer_store_dwordx4 v[20:23], v240, s[52:55], s62 offen offset:1024 sc1\n"
    "s_waitcnt vmcnt(2)\n"
    ".L_KG:\n"
    "s_mul_i32 m0, s32, 4352\n"
    "v_pk_mul_f32 v[168:169], v[52:53], s[90:91] op_sel_hi:[1,0]\n"
    "v_pk_fma_f32 v[170:171], v[52:53], s[90:91], v[168:169] op_sel_hi:[1,0,1] neg_lo:[0,0,1] neg_hi:[0,0,1]\n"
    "v_exp_f32 v168, v168\n"
    "v_exp_f32 v169, v169\n"
    "v_pk_fma_f32 v[170:171], v[52:53], s[92:93], v[170:171] op_sel_hi:[1,0,1]\n"
    "v_pk_mul_f32 v[170:171], v[170:171], s[94:95] op_sel_hi:[1,0]\n"
    "v_pk_fma_f32 v[168:169], v[168:169], v[170:171], v[168:169]\n"
    "v_cvt_pk_bf16_f32 v168, v168, v169\n"
    "v_and_b32 v170, 0xffff0000, v168\n"
    "v_lshlrev_b32 v168, 16, v168\n"
    "v_mul_f32 v168, v168, v170\n"
    "v_mul_f32 v169, v54, v170\n"
    "v_pk_mul_f32 v[56:57], v[56:57], v[170:171] op_sel_hi:[1,0]\n"
    "v_mul_f32 v168, 0xbfb8aa3b, v168\n"
    "v_mul_f32 v169, 0.15915494, v169\n"
    "v_pk_mul_f32 v[56:57], v[56:57], 2.0 op_sel_hi:[1,0] neg_hi:[1,0]\n"
    "v_mul_f32 v172, 0x41000000, v168\n"
    "v_mul_f32 v173, 0x41000000, v169\n"
    "v_mul_f32 v174, 0x43000000, v168\n"
    "v_mul_f32 v175, 0x43000000, v169\n"
    "v_fract_f32 v173, v173\n"
    "v_fract_f32 v169, v169\n"
    "v_fract_f32 v175, v175\n"
    "v_exp_f32 v172, v172\n"
    "v_cos_f32 v162, v173\n"
    "v_sin_f32 v163, v173\n"
    "v_exp_f32 v168, v168\n"
    "v_cos_f32 v160, v169\n"
    "v_sin_f32 v161, v169\n"
    "v_pk_mul_f32 v[162:163], v[162:163], v[172:173] op_sel_hi:[1,0]\n"
    "v_exp_f32 v174, v174\n"
    "v_cos_f32 v164, v175\n"
    "v_sin_f32 v165, v175\n"
    "v_pk_mul_f32 v[160:161], v[160:161], v[168:169] op_sel_hi:[1,0]\n"
    "v_pk_mul_f32 v[166:167], v[162:163], v[162:163] op_sel_hi:[1,0]\n"
    "v_pk_fma_f32 v[166:167], v[162:163], v[162:163], v[166:167] op_sel:[1,1,0] op_sel_hi:[0,1,1] neg_lo:[0,1,0]\n"
    "v_pk_mul_f32 v[164:165], v[164:165], v[174:175] op_sel_hi:[1,0]\n"
    "v_mov_b32 v36, 0x3f80\n"
    "v_cvt_pk_bf16_f32 v37, v162, v163\n"
    "ds_write_addtid_b32 v36 offset:0\n"
    "ds_write_addtid_b32 v37 offset:272\n"
    "v_pk_mul_f32 v[170:171], v[162:163], v[166:167] op_sel_hi:[1,0]\n"
    "v_pk_fma_f32 v[170:171], v[162:163], v[166:167], v[170:171] op_sel:[1,1,0] op_sel_hi:[0,1,1] neg_lo:[0,1,0]\n"
    "v_cvt_pk_bf16_f32 v38, v166, v167\n"
    "v_cvt_pk_bf16_f32 v39, v170, v171\n"
    "ds_write_addtid_b32 v38 offset:544\n"
    "ds_write_addtid_b32 v39 offset:816\n"
    "v_pk_mul_f32 v[172:173], v[166:167], v[166:167] op_sel_hi:[1,0]\n"
    "v_pk_mul_f32 v[174:175], v[170:171], v[166:167] op_sel_hi:[1,0]\n"
    "v_pk_fma_f32 v[172:173], v[166:167], v[166:167], v[172:173] op_sel:[1,1,0] op_sel_hi:[0,1,1] neg_lo:[0,1,0]\n"
    "v_pk_fma_f32 v[174:175], v[170:171], v[166:167], v[174:175] op_sel:[1,1,0] op_sel_hi:[0,1,1] neg_lo:[0,1,0]\n"
    "v_cvt_pk_bf16_f32 v40, v172, v173\n"
    "v_cvt_pk_bf16_f32 v41, v174, v175\n"
    "ds_write_addtid_b32 v40 offset:1088\n"
    "ds_write_addtid_b32 v41 offset:1360\n"
    "v_pk_mul_f32 v[168:169], v[172:173], v[166:167] op_sel_hi:[1,0]\n"
    "v_pk_mul_f32 v[170:171], v[174:175], v[166:167] op_sel_hi:[1,0]\n"
    "v_pk_fma_f32 v[168:169], v[172:173], v[166:167], v[168:169] op_sel:[1,1,0] op_sel_hi:[0,1,1] neg_lo:[0,1,0]\n"
    "v_pk_fma_f32 v[170:171], v[174:175], v[166:167], v[170:171] op_sel:[1,1,0] op_sel_hi:[0,1,1] neg_lo:[0,1,0]\n"
    "v_cvt_pk_bf16_f32 v42, v168, v169\n"
    "v_cvt_pk_bf16_f32 v43, v170, v171\n"
    "ds_write_addtid_b32 v42 offset:1632\n"
    "ds_write_addtid_b32 v43 offset:1904\n"
    "v_pk_mul_f32 v[172:173], v[168:169], v[166:167] op_sel_hi:[1,0]\n"
    "v_pk_mul_f32 v[174:175], v[170:171], v[166:167] op_sel_hi:[1,0]\n"
    "v_pk_fma_f32 v[172:173], v[168:169], v[166:167], v[172:173] op_sel:[1,1,0] op_sel_hi:[0,1,1] neg_lo:[0,1,0]\n"
    "v_pk_fma_f32 v[174:175], v[170:171], v[166:167], v[174:175] op_sel:[1,1,0] op_sel_hi:[0,1,1] neg_lo:[0,1,0]\n"
    "v_cvt_pk_bf16_f32 v36, v172, v173\n"
    "v_cvt_pk_bf16_f32 v37, v174, v175\n"
    "ds_write_addtid_b32 v36 offset:2176\n"
    "ds_write_addtid_b32 v37 offset:2448\n"
    "v_pk_mul_f32 v[168:169], v[172:173], v[166:167] op_sel_hi:[1,0]\n"
    "v_pk_mul_f32 v[170:171], v[174:175], v[166:167] op_sel_hi:[1,0]\n"
    "v_pk_fma_f32 v[168:169], v[172:173], v[166:167], v[168:169] op_sel:[1,1,0] op_sel_hi:[0,1,1] neg_lo:[0,1,0]\n"
    "v_pk_fma_f32 v[170:171], v[174:175], v[166:167], v[170:171] op_sel:[1,1,0] op_sel_hi:[0,1,1] neg_lo:[0,1,0]\n"
    "v_cvt_pk_bf16_f32 v38, v168, v169\n"
    "v_cvt_pk_bf16_f32 v39, v170, v171\n"
    "ds_write_addtid_b32 v38 offset:2720\n"
    "ds_write_addtid_b32 v39 offset:2992\n"
    "v_pk_mul_f32 v[172:173], v[168:169], v[166:167] op_sel_hi:[1,0]\n"
    "v_pk_mul_f32 v[174:175], v[170:171], v[166:167] op_sel_hi:[1,0]\n"
    "v_pk_fma_f32 v[172:173], v[168:169], v[166:167], v[172:173] op_sel:[1,1,0] op_sel_hi:[0,1,1] neg_lo:[0,1,0]\n"
    "v_pk_fma_f32 v[174:175], v[170:171], v[166:167], v[174:175] op_sel:[1,1,0] op_sel_hi:[0,1,1] neg_lo:[0,1,0]\n"
    "v_cvt_pk_bf16_f32 v40, v172, v173\n"
    "v_cvt_pk_bf16_f32 v41, v174, v175\n"
    "ds_write_addtid_b32 v40 offset:3264\n"
    "ds_write_addtid_b32 v41 offset:3536\n"
    "v_pk_mul_f32 v[168:169], v[172:173], v[166:167] op_sel_hi:[1,0]\n"
    "v_pk_mul_f32 v[170:171], v[174:175], v[166:167] op_sel_hi:[1,0]\n"
    "v_pk_fma_f32 v[168:169], v[172:173], v[166:167], v[168:169] op_sel:[1,1,0] op_sel_hi:[0,1,1] neg_lo:[0,1,0]\n"
    "v_pk_fma_f32 v[170:171], v[174:175], v[166:167], v[170:171] op_sel:[1,1,0] op_sel_hi:[0,1,1] neg_lo:[0,1,0]\n"
    "v_cvt_pk_bf16_f32 v42, v168, v169\n"
    "v_cvt_pk_bf16_f32 v43, v170, v171\n"
    "ds_write_addtid_b32 v42 offset:3808\n"
    "ds_write_addtid_b32 v43 offset:4080\n"
    "ds_read_b128 v[188:191], v33 offset:0\n"
    "ds_read_b128 v[192:195], v33 offset:64\n"
    "ds_read_b128 v[196:199], v33 offset:128\n"
    "ds_read_b128 v[200:203], v33 offset:192\n"
    "v_pk_mul_f32 v[176:177], v[56:57], v[164:165] op_sel_hi:[1,0]\n"
    "v_pk_mul_f32 v[168:169], v[56:57], v[160:161] op_sel_hi:[1,0]\n"
    "v_pk_fma_f32 v[176:177], v[56:57], v[164:165], v[176:177] op_sel:[1,1,0] op_sel_hi:[0,1,1] neg_hi:[0,1,0]\n"
    "v_pk_fma_f32 v[168:169], v[56:57], v[160:161], v[168:169] op_sel:[1,1,0] op_sel_hi:[0,1,1] neg_hi:[0,1,0]\n"
    "v_cvt_pk_bf16_f32 v36, v56, v57\n"
    "v_cvt_pk_bf16_f32 v37, v176, v177\n"
    "ds_write_addtid_b32 v36 offset:0\n"
    "ds_write_addtid_b32 v37 offset:272\n"
    "v_pk_mul_f32 v[172:173], v[168:169], v[164:165] op_sel_hi:[1,0]\n"
    "v_pk_mul_f32 v[170:171], v[168:169], v[160:161] op_sel_hi:[1,0]\n"
    "v_pk_fma_f32 v[172:173], v[168:169], v[164:165], v[172:173] op_sel:[1,1,0] op_sel_hi:[0,1,1] neg_hi:[0,1,0]\n"
    "v_pk_fma_f32 v[170:171], v[168:169], v[160:161], v[170:171] op_sel:[1,1,0] op_sel_hi:[0,1,1] neg_hi:[0,1,0]\n"
    "v_cvt_pk_bf16_f32 v38, v168, v169\n"
    "v_cvt_pk_bf16_f32 v39, v172, v173\n"
    "ds_write_addtid_b32 v38 offset:544\n"
    "ds_write_addtid_b32 v39 offset:816\n"
    "v_pk_mul_f32 v[178:179], v[170:171], v[164:165] op_sel_hi:[1,0]\n"
    "v_pk_mul_f32 v[168:169], v[170:171], v[160:161] op_sel_hi:[1,0]\n"
    "v_pk_fma_f32 v[178:179], v[170:171], v[164:165], v[178:179] op_sel:[1,1,0] op_sel_hi:[0,1,1] neg_hi:[0,1,0]\n"
    "v_pk_fma_f32 v[168:169], v[170:171], v[160:161], v[168:169] op_sel:[1,1,0] op_sel_hi:[0,1,1] neg_hi:[0,1,0]\n"
    "v_cvt_pk_bf16_f32 v40, v170, v171\n"
    "v_cvt_pk_bf16_f32 v41, v178, v179\n"
    "ds_write_addtid_b32 v40 offset:1088\n"
    "ds_write_addtid_b32 v41 offset:1360\n"
    "v_pk_mul_f32 v[172:173], v[168:169], v[164:165] op_sel_hi:[1,0]\n"
    "v_pk_mul_f32 v[170:171], v[168:169], v[160:161] op_sel_hi:[1,0]\n"
    "v_pk_fma_f32 v[172:173], v[168:169], v[164:165], v[172:173] op_sel:[1,1,0] op_sel_hi:[0,1,1] neg_hi:[0,1,0]\n"
    "v_pk_fma_f32 v[170:171], v[168:169], v[160:161], v[170:171] op_sel:[1,1,0] op_sel_hi:[0,1,1] neg_hi:[0,1,0]\n"
    "v_cvt_pk_bf16_f32 v42, v168, v169\n"
    "v_cvt_pk_bf16_f32 v43, v172, v173\n"
    "ds_write_addtid_b32 v42 offset:1632\n"
    "ds_write_addtid_b32 v43 offset:1904\n"
    "s_cmp_ge_u32 s32, 4\n"
    "s_cbranch_scc1 .L_FP0\n"
    "s_waitcnt vmcnt(0)\n"
    "ds_write_b32 v250, v250 offset:96\n"
    "s_cmp_lg_u32 s32, 3\n"
    "s_cbranch_scc1 .L_FQ0\n"
    "s_mov_b32 s60, 1048576\n"
    ".L_SD0:\n"
    "ds_read_b32 v252, v251 offset:96\n"
    "s_waitcnt lgkmcnt(0)\n"
    "v_cmp_eq_u32 vcc, 0, v252\n"
    "s_cbranch_vccz .L_SD0OK\n"
    "s_sub_u32 s60, s60, 1\n"
    "s_cbranch_scc0 .L_SD0\n"
    ".L_SD0OK:\n"
    "s_lshl_b32 s58, s2, 7\n"
    "v_mov_b32 v8, s40\n"
    "s_add_u32 s58, s58, 4096\n"
    "s_mov_b64 exec, 1\n"
    "buffer_store_dword v8, off, s[52:55], s58 sc1\n"
    "s_mov_b64 exec, -1\n"
    ".L_FQ0:\n"
    ".L_FP0:\n"
    "v_pk_mul_f32 v[180:181], v[170:171], v[164:165] op_sel_hi:[1,0]\n"
    "v_pk_mul_f32 v[168:169], v[170:171], v[160:161] op_sel_hi:[1,0]\n"
    "v_pk_fma_f32 v[180:181], v[170:171], v[164:165], v[180:181] op_sel:[1,1,0] op_sel_hi:[0,1,1] neg_hi:[0,1,0]\n"
    "v_pk_fma_f32 v[168:169], v[170:171], v[160:161], v[168:169] op_sel:[1,1,0] op_sel_hi:[0,1,1] neg_hi:[0,1,0]\n"
    "v_cvt_pk_bf16_f32 v36, v170, v171\n"
    "v_cvt_pk_bf16_f32 v37, v180, v181\n"
    "ds_write_addtid_b32 v36 offset:2176\n"
    "ds_write_addtid_b32 v37 offset:2448\n"
    "v_pk_mul_f32 v[172:173], v[168:169], v[164:165] op_sel_hi:[1,0]\n"
    "v_pk_mul_f32 v[170:171], v[168:169], v[160:161] op_sel_hi:[1,0]\n"
    "v_pk_fma_f32 v[172:173], v[168:169], v[164:165], v[172:173] op_sel:[1,1,0] op_sel_hi:[0,1,1] neg_hi:[0,1,0]\n"
    "v_pk_fma_f32 v[170:171], v[168:169], v[160:161], v[170:171] op_sel:[1,1,0] op_sel_hi:[0,1,1] neg_hi:[0,1,0]\n"
    "v_cvt_pk_bf16_f32 v38, v168, v169\n"
    "v_cvt_pk_bf16_f32 v39, v172, v173\n"
    "ds_write_addtid_b32 v38 offset:2720\n"
    "ds_write_addtid_b32 v39 offset:2992\n"
    "v_pk_mul_f32 v[182:183], v[170:171], v[164:165] op_sel_hi:[1,0]\n"
    "v_pk_mul_f32 v[168:169], v[170:171], v[160:161] op_sel_hi:[1,0]\n"
    "v_pk_fma_f32 v[182:183], v[170:171], v[164:165], v[182:183] op_sel:[1,1,0] op_sel_hi:[0,1,1] neg_hi:[0,1,0]\n"
    "v_pk_fma_f32 v[168:169], v[170:171], v[160:161], v[168:169] op_sel:[1,1,0] op_sel_hi:[0,1,1] neg_hi:[0,1,0]\n"
    "v_cvt_pk_bf16_f32 v40, v170, v171\n"
    "v_cvt_pk_bf16_f32 v41, v182, v183\n"
    "ds_write_addtid_b32 v40 offset:3264\n"
    "ds_write_addtid_b32 v41 offset:3536\n"
    "v_pk_mul_f32 v[172:173], v[168:169], v[164:165] op_sel_hi:[1,0]\n"
    "v_pk_fma_f32 v[172:173], v[168:169], v[164:165], v[172:173] op_sel:[1,1,0] op_sel_hi:[0,1,1] neg_hi:[0,1,0]\n"
    "v_cvt_pk_bf16_f32 v42, v168, v169\n"
    "v_cvt_pk_bf16_f32 v43, v172, v173\n"
    "ds_write_addtid_b32 v42 offset:3808\n"
    "ds_write_addtid_b32 v43 offset:4080\n"
    "ds_read_b128 v[204:207], v33 offset:0\n"
    "ds_read_b128 v[208:211], v33 offset:64\n"
    "ds_read_b128 v[212:215], v33 offset:128\n"
    "ds_read_b128 v[216:219], v33 offset:192\n"
    "v_pk_mul_f32 v[168:169], v[176:177], v[164:165] op_sel_hi:[1,0]\n"
    "v_pk_mul_f32 v[170:171], v[178:179], v[164:165] op_sel_hi:[1,0]\n"
    "v_pk_fma_f32 v[168:169], v[176:177], v[164:165], v[168:169] op_sel:[1,1,0] op_sel_hi:[0,1,1] neg_hi:[0,1,0]\n"
    "v_pk_fma_f32 v[170:171], v[178:179], v[164:165], v[170:171] op_sel:[1,1,0] op_sel_hi:[0,1,1] neg_hi:[0,1,0]\n"
    "v_cvt_pk_bf16_f32 v36, v168, v169\n"
    "v_cvt_pk_bf16_f32 v37, v170, v171\n"
    "ds_write_addtid_b32 v36 offset:0\n"
    "ds_write_addtid_b32 v37 offset:272\n"
    "v_pk_mul_f32 v[172:173], v[180:181], v[164:165] op_sel_hi:[1,0]\n"
    "v_pk_mul_f32 v[174:175], v[182:183], v[164:165] op_sel_hi:[1,0]\n"
    "v_pk_fma_f32 v[172:173], v[180:181], v[164:165], v[172:173] op_sel:[1,1,0] op_sel_hi:[0,1,1] neg_hi:[0,1,0]\n"
    "v_pk_fma_f32 v[174:175], v[182:183], v[164:165], v[174:175] op_sel:[1,1,0] op_sel_hi:[0,1,1] neg_hi:[0,1,0]\n"
    "v_cvt_pk_bf16_f32 v38, v172, v173\n"
    "v_cvt_pk_bf16_f32 v39, v174, v175\n"
    "ds_write_addtid_b32 v38 offset:544\n"
    "ds_write_addtid_b32 v39 offset:816\n"
    "s_waitcnt lgkmcnt(4)\n"
    "v_mfma_f32_16x16x32_bf16 v[220:223], v[204:207], v[188:191], 0\n"
    "v_mfma_f32_16x16x32_bf16 v[220:223], v[208:211], v[192:195], v[220:223]\n"
    "v_mfma_f32_16x16x32_bf16 v[220:223], v[212:215], v[196:199], v[220:223]\n"
    "v_mfma_f32_16x16x32_bf16 v[220:223], v[216:219], v[200:203], v[220:223]\n"
    "v_pk_mul_f32 v[176:177], v[168:169], v[164:165] op_sel_hi:[1,0]\n"
    "v_pk_mul_f32 v[178:179], v[170:171], v[164:165] op_sel_hi:[1,0]\n"
    "v_pk_fma_f32 v[176:177], v[168:169], v[164:165], v[176:177] op_sel:[1,1,0] op_sel_hi:[0,1,1] neg_hi:[0,1,0]\n"
    "v_pk_fma_f32 v[178:179], v[170:171], v[164:165], v[178:179] op_sel:[1,1,0] op_sel_hi:[0,1,1] neg_hi:[0,1,0]\n"
    "v_cvt_pk_bf16_f32 v40, v176, v177\n"
    "v_cvt_pk_bf16_f32 v41, v178, v179\n"
    "ds_write_addtid_b32 v40 offset:1088\n"
    "ds_write_addtid_b32 v41 offset:1360\n"
    "v_pk_mul_f32 v[180:181], v[172:173], v[164:165] op_sel_hi:[1,0]\n"
    "v_pk_mul_f32 v[182:183], v[174:175], v[164:165] op_sel_hi:[1,0]\n"
    "v_pk_fma_f32 v[180:181], v[172:173], v[164:165], v[180:181] op_sel:[1,1,0] op_sel_hi:[0,1,1] neg_hi:[0,1,0]\n"
    "v_pk_fma_f32 v[182:183], v[174:175], v[164:165], v[182:183] op_sel:[1,1,0] op_sel_hi:[0,1,1] neg_hi:[0,1,0]\n"
    "v_cvt_pk_bf16_f32 v42, v180, v181\n"
    "v_cvt_pk_bf16_f32 v43, v182, v183\n"
    "ds_write_addtid_b32 v42 offset:1632\n"
    "ds_write_addtid_b32 v43 offset:1904\n"
    "v_pk_mul_f32 v[168:169], v[176:177], v[164:165] op_sel_hi:[1,0]\n"
    "v_pk_mul_f32 v[170:171], v[180:181], v[164:165] op_sel_hi:[1,0]\n"
    "v_pk_fma_f32 v[168:169], v[176:177], v[164:165], v[168:169] op_sel:[1,1,0] op_sel_hi:[0,1,1] neg_hi:[0,1,0]\n"
    "v_pk_fma_f32 v[170:171], v[180:181], v[164:165], v[170:171] op_sel:[1,1,0] op_sel_hi:[0,1,1] neg_hi:[0,1,0]\n"
    "v_cvt_pk_bf16_f32 v36, v168, v169\n"
    "v_cvt_pk_bf16_f32 v37, v170, v171\n"
    "ds_write_addtid_b32 v36 offset:2176\n"
    "ds_write_addtid_b32 v37 offset:2448\n"
    "v_pk_mul_f32 v[172:173], v[168:169], v[164:165] op_sel_hi:[1,0]\n"
    "v_pk_mul_f32 v[174:175], v[170:171], v[164:165] op_sel_hi:[1,0]\n"
    "v_pk_fma_f32 v[172:173], v[168:169], v[164:165], v[172:173] op_sel:[1,1,0] op_sel_hi:[0,1,1] neg_hi:[0,1,0]\n"
    "v_pk_fma_f32 v[174:175], v[170:171], v[164:165], v[174:175] op_sel:[1,1,0] op_sel_hi:[0,1,1] neg_hi:[0,1,0]\n"
    "v_cvt_pk_bf16_f32 v38, v172, v173\n"
    "v_cvt_pk_bf16_f32 v39, v174, v175\n"
    "ds_write_addtid_b32 v38 offset:2720\n"
    "ds_write_addtid_b32 v39 offset:2992\n"
    "v_pk_mul_f32 v[168:169], v[172:173], v[164:165] op_sel_hi:[1,0]\n"
    "v_pk_mul_f32 v[170:171], v[174:175], v[164:165] op_sel_hi:[1,0]\n"
    "v_pk_fma_f32 v[168:169], v[172:173], v[164:165], v[168:169] op_sel:[1,1,0] op_sel_hi:[0,1,1] neg_hi:[0,1,0]\n"
    "v_pk_fma_f32 v[170:171], v[174:175], v[164:165], v[170:171] op_sel:[1,1,0] op_sel_hi:[0,1,1] neg_hi:[0,1,0]\n"
    "v_cvt_pk_bf16_f32 v40, v168, v169\n"
    "v_cvt_pk_bf16_f32 v41, v170, v171\n"
    "ds_write_addtid_b32 v40 offset:3264\n"
    "ds_write_addtid_b32 v41 offset:3536\n"
    "v_pk_mul_f32 v[172:173], v[168:169], v[164:165] op_sel_hi:[1,0]\n"
    "v_pk_mul_f32 v[174:175], v[170:171], v[164:165] op_sel_hi:[1,0]\n"
    "v_pk_fma_f32 v[172:173], v[168:169], v[164:165], v[172:173] op_sel:[1,1,0] op_sel_hi:[0,1,1] neg_hi:[0,1,0]\n"
    "v_pk_fma_f32 v[174:175], v[170:171], v[164:165], v[174:175] op_sel:[1,1,0] op_sel_hi:[0,1,1] neg_hi:[0,1,0]\n"
    "v_cvt_pk_bf16_f32 v42, v172, v173\n"
    "v_cvt_pk_bf16_f32 v43, v174, v175\n"
    "ds_write_addtid_b32 v42 offset:3808\n"
    "ds_write_addtid_b32 v43 offset:4080\n"
    "ds_read_b128 v[204:207], v33 offset:0\n"
    "ds_read_b128 v[208:211], v33 offset:64\n"
    "ds_read_b128 v[212:215], v33 offset:128\n"
    "ds_read_b128 v[216:219], v33 offset:192\n"
    "v_pk_mul_f32 v[166:167], v[164:165], v[164:165] op_sel_hi:[1,0]\n"
    "v_pk_fma_f32 v[166:167], v[164:165], v[164:165], v[166:167] op_sel:[1,1,0] op_sel_hi:[0,1,1] neg_lo:[0,1,0]\n"
    "v_pk_mul_f32 v[168:169], v[172:173], v[164:165] op_sel_hi:[1,0]\n"
    "v_pk_mul_f32 v[170:171], v[172:173], v[166:167] op_sel_hi:[1,0]\n"
    "v_pk_fma_f32 v[168:169], v[172:173], v[164:165], v[168:169] op_sel:[1,1,0] op_sel_hi:[0,1,1] neg_hi:[0,1,0]\n"
    "v_pk_fma_f32 v[170:171], v[172:173], v[166:167], v[170:171] op_sel:[1,1,0] op_sel_hi:[0,1,1] neg_hi:[0,1,0]\n"
    "v_cvt_pk_bf16_f32 v36, v168, v169\n"
    "v_cvt_pk_bf16_f32 v37, v170, v171\n"
    "ds_write_addtid_b32 v36 offset:0\n"
    "ds_write_addtid_b32 v37 offset:272\n"
    "v_pk_mul_f32 v[176:177], v[168:169], v[166:167] op_sel_hi:[1,0]\n"
    "v_pk_mul_f32 v[178:179], v[170:171], v[166:167] op_sel_hi:[1,0]\n"
    "v_pk_fma_f32 v[176:177], v[168:169], v[166:167], v[176:177] op_sel:[1,1,0] op_sel_hi:[0,1,1] neg_hi:[0,1,0]\n"
    "v_pk_fma_f32 v[178:179], v[170:171], v[166:167], v[178:179] op_sel:[1,1,0] op_sel_hi:[0,1,1] neg_hi:[0,1,0]\n"
    "v_cvt_pk_bf16_f32 v38, v176, v177\n"
    "v_cvt_pk_bf16_f32 v39, v178, v179\n"
    "ds_write_addtid_b32 v38 offset:544\n"
    "ds_write_addtid_b32 v39 offset:816\n"
    "s_waitcnt lgkmcnt(4)\n"
    "v_mfma_f32_16x16x32_bf16 v[224:227], v[204:207], v[188:191], 0\n"
    "v_mfma_f32_16x16x32_bf16 v[224:227], v[208:211], v[192:195], v[224:227]\n"
    "v_mfma_f32_16x16x32_bf16 v[224:227], v[212:215], v[196:199], v[224:227]\n"
    "v_mfma_f32_16x16x32_bf16 v[224:227], v[216:219], v[200:203], v[224:227]\n"
    "v_pk_mul_f32 v[168:169], v[176:177], v[166:167] op_sel_hi:[1,0]\n"
    "v_pk_mul_f32 v[170:171], v[178:179], v[166:167] op_sel_hi:[1,0]\n"
    "v_pk_fma_f32 v[168:169], v[176:177], v[166:167], v[168:169] op_sel:[1,1,0] op_sel_hi:[0,1,1] neg_hi:[0,1,0]\n"
    "v_pk_fma_f32 v[170:171], v[178:179], v[166:167], v[170:171] op_sel:[1,1,0] op_sel_hi:[0,1,1] neg_hi:[0,1,0]\n"
    "v_cvt_pk_bf16_f32 v40, v168, v169\n"
    "v_cvt_pk_bf16_f32 v41, v170, v171\n"
    "ds_write_addtid_b32 v40 offset:1088\n"
    "ds_write_addtid_b32 v41 offset:1360\n"
    "v_pk_mul_f32 v[176:177], v[168:169], v[166:167] op_sel_hi:[1,0]\n"
    "v_pk_mul_f32 v[178:179], v[170:171], v[166:167] op_sel_hi:[1,0]\n"
    "v_pk_fma_f32 v[176:177], v[168:169], v[166:167], v[176:177] op_sel:[1,1,0] op_sel_hi:[0,1,1] neg_hi:[0,1,0]\n"
    "v_pk_fma_f32 v[178:179], v[170:171], v[166:167], v[178:179] op_sel:[1,1,0] op_sel_hi:[0,1,1] neg_hi:[0,1,0]\n"
    "v_cvt_pk_bf16_f32 v42, v176, v177\n"
    "v_cvt_pk_bf16_f32 v43, v178, v179\n"
    "ds_write_addtid_b32 v42 offset:1632\n"
    "ds_write_addtid_b32 v43 offset:1904\n"
    "v_pk_mul_f32 v[168:169], v[176:177], v[166:167] op_sel_hi:[1,0]\n"
    "v_pk_fma_f32 v[168:169], v[176:177], v[166:167], v[168:169] op_sel:[1,1,0] op_sel_hi:[0,1,1] neg_hi:[0,1,0]\n"
    "v_cvt_pk_bf16_f32 v36, v168, v169\n"
    "ds_write_addtid_b32 v36 offset:2176\n"
    "ds_read_b128 v[204:207], v33 offset:0\n"
    "ds_read_b128 v[208:211], v33 offset:64\n"
    "ds_read_b128 v[212:215], v33 offset:128\n"
    "ds_read_b128 v[216:219], v33 offset:192\n"
    "s_cmp_ge_u32 s32, 4\n"
    "s_cbranch_scc1 .L_PPE\n"
    "buffer_load_dword v16, v13, s[52:55], 0 offen sc1\n"
    ".L_PPE:\n"
    "ds_write_b128 v34, v[220:223]\n"
    "ds_write_b128 v34, v[224:227] offset:1024\n"
    "s_waitcnt lgkmcnt(2)\n"
    "v_mfma_f32_16x16x32_bf16 v[228:231], v[204:207], v[188:191], 0\n"
    "v_mfma_f32_16x16x32_bf16 v[228:231], v[208:211], v[192:195], v[228:231]\n"
    "v_mfma_f32_16x16x32_bf16 v[228:231], v[212:215], v[196:199], v[228:231]\n"
    "v_mfma_f32_16x16x32_bf16 v[228:231], v[216:219], v[200:203], v[228:231]\n"
    "s_cmp_ge_u32 s32, 4\n"
    "s_cbranch_scc1 .L_H2\n"
    "s_nop 5\n"
    "ds_write_b128 v34, v[228:231] offset:2048\n"
    "ds_write_b32 v250, v250 offset:112\n"
    "s_setprio 3\n"
    ".L_AP:\n"
    "s_mov_b32 s60, 1048576\n"
    "s_branch .L_P1C\n"
    ".L_P1:\n"
    "buffer_load_dword v16, v13, s[52:55], 0 offen sc1\n"
    ".L_P1C:\n"
    "s_waitcnt vmcnt(0)\n"
    "v_cmp_ne_u32 vcc, s40, v16\n"
    "s_cbranch_vccz .L_P1OK\n"
    "s_sub_u32 s60, s60, 1\n"
    "s_cbranch_scc0 .L_P1\n"
    ".L_P1OK:\n"
    "s_mov_b32 m0, s68\n"
    "s_nop 0\n"
    "buffer_load_dwordx4 v2, s[52:55], s69 offen sc1 lds\n"
    "s_mov_b32 m0, s70\n"
    "s_nop 0\n"
    "buffer_load_dwordx4 v2, s[52:55], s71 offen sc1 lds\n"
    "buffer_load_dwordx4 v[160:163], v11, s[52:55], 0 offen offset:0 sc1\n"
    "buffer_load_dwordx4 v[164:167], v11, s[52:55], 0 offen offset:1024 sc1\n"
    "buffer_load_dwordx4 v[168:171], v11, s[52:55], 0 offen offset:2048 sc1\n"
    "buffer_load_dwordx4 v[172:175], v11, s[52:55], 0 offen offset:3072 sc1\n"
    "buffer_load_dwordx4 v[176:179], v11, s[52:55], s73 offen offset:0 sc1\n"
    "buffer_load_dwordx4 v[180:183], v11, s[52:55], s73 offen offset:1024 sc1\n"
    "buffer_load_dwordx4 v[184:187], v11, s[52:55], s73 offen offset:2048 sc1\n"
    "buffer_load_dwordx4 v[188:191], v11, s[52:55], s73 offen offset:3072 sc1\n"
    "buffer_load_dword v15, v14, s[52:55], 0 offen sc1\n"
    "s_waitcnt vmcnt(9)\n"
    "ds_write_b32 v250, v250 offset:0\n"
    "s_mov_b32 s60, 1048576\n"
    ".L_SY0:\n"
    "ds_read_b32 v252, v251 offset:0\n"
    "s_waitcnt lgkmcnt(0)\n"
    "v_cmp_eq_u32 vcc, 0, v252\n"
    "s_cbranch_vccz .L_SY0OK\n"
    "s_sub_u32 s60, s60, 1\n"
    "s_cbranch_scc0 .L_SY0\n"
    ".L_SY0OK:\n"
    "ds_read2st64_b64 v[128:131], v28 offset0:80 offset1:81\n"
    "ds_read2st64_b64 v[132:135], v28 offset0:82 offset1:83\n"
    "ds_read2st64_b64 v[136:139], v29 offset0:80 offset1:81\n"
    "ds_read2st64_b64 v[140:143], v29 offset0:82 offset1:83\n"
    "ds_read2st64_b64 v[144:147], v30 offset0:80 offset1:81\n"
    "ds_read2st64_b64 v[148:151], v30 offset0:82 offset1:83\n"
    "ds_read2st64_b64 v[152:155], v31 offset0:80 offset1:81\n"
    "ds_read2st64_b64 v[156:159], v31 offset0:82 offset1:83\n"
    "buffer_load_dwordx4 v[192:195], v11, s[52:55], s74 offen offset:0 sc1\n"
    "buffer_load_dwordx4 v[196:199], v11, s[52:55], s74 offen offset:1024 sc1\n"
    "buffer_load_dwordx4 v[200:203], v11, s[52:55], s74 offen offset:2048 sc1\n"
    "buffer_load_dwordx4 v[204:207], v11, s[52:55], s74 offen offset:3072 sc1\n"
    "s_waitcnt vmcnt(11)\n"
    "s_waitcnt lgkmcnt(6)\n"
    "v_mfma_f32_32x32x16_bf16 v[16:31], v[160:163], v[128:131], 0\n"
    "v_mfma_f32_32x32x16_bf16 v[16:31], v[164:167], v[132:135], v[16:31]\n"
    "buffer_load_dwordx4 v[208:211], v11, s[52:55], s75 offen offset:0 sc1\n"
    "buffer_load_dwordx4 v[212:215], v11, s[52:55], s75 offen offset:1024 sc1\n"
    "buffer_load_dwordx4 v[216:219], v11, s[52:55], s75 offen offset:2048 sc1\n"
    "buffer_load_dwordx4 v[220:223], v11, s[52:55], s75 offen offset:3072 sc1\n"
    "ds_write_b32 v250, v250 offset:144\n"
    "s_setprio 0\n"
    "s_waitcnt lgkmcnt(1)\n"
    "v_mfma_f32_32x32x16_bf16 v[32:47], v[160:163], v[136:139], 0\n"
    "s_nop 3\n"
    "v_exp_f32 v48, v16\n"
    "v_exp_f32 v49, v17\n"
    "v_exp_f32 v50, v18\n"
    "v_exp_f32 v51, v19\n"
    "v_mfma_f32_32x32x16_bf16 v[32:47], v[164:167], v[140:143], v[32:47]\n"
    "ds_write_b32 v250, v250 offset:272\n"
    "v_exp_f32 v52, v20\n"
    "v_exp_f32 v53, v21\n"
    "v_exp_f32 v54, v22\n"
    "v_exp_f32 v55, v23\n"
    "v_exp_f32 v56, v24\n"
    "v_exp_f32 v57, v25\n"
    "v_exp_f32 v58, v26\n"
    "v_exp_f32 v59, v27\n"
    "s_waitcnt vmcnt(14)\n"
    "v_smfmac_f32_32x32x32_bf16 v[64:79], v[168:171], v[48:55], v10\n"
    "v_exp_f32 v60, v28\n"
    "v_exp_f32 v61, v29\n"
    "v_exp_f32 v62, v30\n"
    "v_exp_f32 v63, v31\n"
    "v_mfma_f32_32x32x16_bf16 v[16:31], v[160:163], v[144:147], 0\n"
    "v_exp_f32 v48, v32\n"
    "v_exp_f32 v49, v33\n"
    "v_exp_f32 v50, v34\n"
    "v_exp_f32 v51, v35\n"
    "v_mfma_f32_32x32x16_bf16 v[16:31], v[164:167], v[148:151], v[16:31]\n"
    "v_exp_f32 v52, v36\n"
    "v_exp_f32 v53, v37\n"
    "v_exp_f32 v54, v38\n"
    "v_exp_f32 v55, v39\n"
    "s_waitcnt vmcnt(13)\n"
    "v_smfmac_f32_32x32x32_bf16 v[64:79], v[172:175], v[56:63], v10\n"
    "v_exp_f32 v56, v40\n"
    "v_exp_f32 v57, v41\n"
    "v_exp_f32 v58, v42\n"
    "v_exp_f32 v59, v43\n"
    "v_smfmac_f32_32x32x32_bf16 v[80:95], v[168:171], v[48:55], v10\n"
    "v_exp_f32 v60, v44\n"
    "v_exp_f32 v61, v45\n"
    "v_exp_f32 v62, v46\n"
    "v_exp_f32 v63, v47\n"
    "v_mfma_f32_32x32x16_bf16 v[32:47], v[160:163], v[152:155], 0\n"
    "v_exp_f32 v48, v16\n"
    "v_exp_f32 v49, v17\n"
    "v_exp_f32 v50, v18\n"
    "v_exp_f32 v51, v19\n"
    "v_mfma_f32_32x32x16_bf16 v[32:47], v[164:167], v[156:159], v[32:47]\n"
    "v_exp_f32 v52, v20\n"
    "v_exp_f32 v53, v21\n"
    "v_exp_f32 v54, v22\n"
    "v_exp_f32 v55, v23\n"
    "v_smfmac_f32_32x32x32_bf16 v[80:95], v[172:175], v[56:63], v10\n"
    "v_exp_f32 v56, v24\n"
    "v_exp_f32 v57, v25\n"
    "v_exp_f32 v58, v26\n"
    "v_exp_f32 v59, v27\n"
    "v_smfmac_f32_32x32x32_bf16 v[96:111], v[168:171], v[48:55], v10\n"
    "v_exp_f32 v60, v28\n"
    "v_exp_f32 v61, v29\n"
    "v_exp_f32 v62, v30\n"
    "v_exp_f32 v63, v31\n"
    "s_waitcnt vmcnt(9)\n"
    "v_mfma_f32_32x32x16_bf16 v[16:31], v[176:179], v[128:131], 0\n"
    "v_exp_f32 v48, v32\n"
    "v_exp_f32 v49, v33\n"
    "v_exp_f32 v50, v34\n"
    "v_exp_f32 v51, v35\n"
    "v_mfma_f32_32x32x16_bf16 v[16:31], v[180:183], v[132:135], v[16:31]\n"
    "v_exp_f32 v52, v36\n"
    "v_exp_f32 v53, v37\n"
    "v_exp_f32 v54, v38\n"
    "v_exp_f32 v55, v39\n"
    "v_smfmac_f32_32x32x32_bf16 v[96:111], v[172:175], v[56:63], v10\n"
    "v_exp_f32 v56, v40\n"
    "v_exp_f32 v57, v41\n"
    "v_exp_f32 v58, v42\n"
    "v_exp_f32 v59, v43\n"
    "v_smfmac_f32_32x32x32_bf16 v[112:127], v[168:171], v[48:55], v10\n"
    "v_exp_f32 v60, v44\n"
    "v_exp_f32 v61, v45\n"
    "v_exp_f32 v62, v46\n"
    "v_exp_f32 v63, v47\n"
    "v_mfma_f32_32x32x16_bf16 v[32:47], v[176:179], v[136:139], 0\n"
    "v_exp_f32 v48, v16\n"
    "v_exp_f32 v49, v17\n"
    "v_exp_f32 v50, v18\n"
    "v_exp_f32 v51, v19\n"
    "v_mfma_f32_32x32x16_bf16 v[32:47], v[180:183], v[140:143], v[32:47]\n"
    "v_exp_f32 v52, v20\n"
    "v_exp_f32 v53, v21\n"
    "v_exp_f32 v54, v22\n"
    "v_exp_f32 v55, v23\n"
    "v_smfmac_f32_32x32x32_bf16 v[112:127], v[172:175], v[56:63], v10\n"
    "v_exp_f32 v56, v24\n"
    "v_exp_f32 v57, v25\n"
    "v_exp_f32 v58, v26\n"
    "v_exp_f32 v59, v27\n"
    "v_smfmac_f32_32x32x32_bf16 v[64:79], v[184:187], v[48:55], v10\n"
    "v_exp_f32 v60, v28\n"
    "v_exp_f32 v61, v29\n"
    "v_exp_f32 v62, v30\n"
    "v_exp_f32 v63, v31\n"
    "v_mfma_f32_32x32x16_bf16 v[16:31], v[176:179], v[144:147], 0\n"
    "v_exp_f32 v48, v32\n"
    "v_exp_f32 v49, v33\n"
    "v_exp_f32 v50, v34\n"
    "v_exp_f32 v51, v35\n"
    "v_mfma_f32_32x32x16_bf16 v[16:31], v[180:183], v[148:151], v[16:31]\n"
    "v_exp_f32 v52, v36\n"
    "v_exp_f32 v53, v37\n"
    "v_exp_f32 v54, v38\n"
    "v_exp_f32 v55, v39\n"
    "v_smfmac_f32_32x32x32_bf16 v[64:79], v[188:191], v[56:63], v10\n"
    "v_exp_f32 v56, v40\n"
    "v_exp_f32 v57, v41\n"
    "v_exp_f32 v58, v42\n"
    "v_exp_f32 v59, v43\n"
    "v_smfmac_f32_32x32x32_bf16 v[80:95], v[184:187], v[48:55], v10\n"
    "v_exp_f32 v60, v44\n"
    "v_exp_f32 v61, v45\n"
    "v_exp_f32 v62, v46\n"
    "v_exp_f32 v63, v47\n"
    "v_mfma_f32_32x32x16_bf16 v[32:47], v[176:179], v[152:155], 0\n"
    "v_exp_f32 v48, v16\n"
    "v_exp_f32 v49, v17\n"
    "v_exp_f32 v50, v18\n"
    "v_exp_f32 v51, v19\n"
    "v_mfma_f32_32x32x16_bf16 v[32:47], v[180:183], v[156:159], v[32:47]\n"
    "v_exp_f32 v52, v20\n"
    "v_exp_f32 v53, v21\n"
    "v_exp_f32 v54, v22\n"
    "v_exp_f32 v55, v23\n"
    "v_smfmac_f32_32x32x32_bf16 v[80:95], v[188:191], v[56:63], v10\n"
    "v_exp_f32 v56, v24\n"
    "v_exp_f32 v57, v25\n"
    "v_exp_f32 v58, v26\n"
    "v_exp_f32 v59, v27\n"
    "v_smfmac_f32_32x32x32_bf16 v[96:111], v[184:187], v[48:55], v10\n"
    "v_exp_f32 v60, v28\n"
    "v_exp_f32 v61, v29\n"
    "v_exp_f32 v62, v30\n"
    "v_exp_f32 v63, v31\n"
    "s_waitcnt vmcnt(4)\n"
    "v_mfma_f32_32x32x16_bf16 v[16:31], v[192:195], v[128:131], 0\n"
    "v_exp_f32 v48, v32\n"
    "v_exp_f32 v49, v33\n"
    "v_exp_f32 v50, v34\n"
    "v_exp_f32 v51, v35\n"
    "v_mfma_f32_32x32x16_bf16 v[16:31], v[196:199], v[132:135], v[16:31]\n"
    "v_exp_f32 v52, v36\n"
    "v_exp_f32 v53, v37\n"
    "v_exp_f32 v54, v38\n"
    "v_exp_f32 v55, v39\n"
    "v_smfmac_f32_32x32x32_bf16 v[96:111], v[188:191], v[56:63], v10\n"
    "v_exp_f32 v56, v40\n"
    "v_exp_f32 v57, v41\n"
    "v_exp_f32 v58, v42\n"
    "v_exp_f32 v59, v43\n"
    "v_smfmac_f32_32x32x32_bf16 v[112:127], v[184:187], v[48:55], v10\n"
    "v_exp_f32 v60, v44\n"
    "v_exp_f32 v61, v45\n"
    "v_exp_f32 v62, v46\n"
    "v_exp_f32 v63, v47\n"
    "v_mfma_f32_32x32x16_bf16 v[32:47], v[192:195], v[136:139], 0\n"
    "v_exp_f32 v48, v16\n"
    "v_exp_f32 v49, v17\n"
    "v_exp_f32 v50, v18\n"
    "v_exp_f32 v51, v19\n"
    "v_mfma_f32_32x32x16_bf16 v[32:47], v[196:199], v[140:143], v[32:47]\n"
    "v_exp_f32 v52, v20\n"
    "v_exp_f32 v53, v21\n"
    "v_exp_f32 v54, v22\n"
    "v_exp_f32 v55, v23\n"
    "v_smfmac_f32_32x32x32_bf16 v[112:127], v[188:191], v[56:63], v10\n"
    "v_exp_f32 v56, v24\n"
    "v_exp_f32 v57, v25\n"
    "v_exp_f32 v58, v26\n"
    "v_exp_f32 v59, v27\n"
    "v_smfmac_f32_32x32x32_bf16 v[64:79], v[200:203], v[48:55], v10\n"
    "v_exp_f32 v60, v28\n"
    "v_exp_f32 v61, v29\n"
    "v_exp_f32 v62, v30\n"
    "v_exp_f32 v63, v31\n"
    "v_mfma_f32_32x32x16_bf16 v[16:31], v[192:195], v[144:147], 0\n"
    "v_exp_f32 v48, v32\n"
    "v_exp_f32 v49, v33\n"
    "v_exp_f32 v50, v34\n"
    "v_exp_f32 v51, v35\n"
    "v_mfma_f32_32x32x16_bf16 v[16:31], v[196:199], v[148:151], v[16:31]\n"
    "v_exp_f32 v52, v36\n"
    "v_exp_f32 v53, v37\n"
    "v_exp_f32 v54, v38\n"
    "v_exp_f32 v55, v39\n"
    "v_smfmac_f32_32x32x32_bf16 v[64:79], v[204:207], v[56:63], v10\n"
    "ds_read_b32 v252, v250 offset:160\n"
    "v_exp_f32 v56, v40\n"
    "v_exp_f32 v57, v41\n"
    "v_exp_f32 v58, v42\n"
    "v_exp_f32 v59, v43\n"
    "v_smfmac_f32_32x32x32_bf16 v[80:95], v[200:203], v[48:55], v10\n"
    "v_exp_f32 v60, v44\n"
    "v_exp_f32 v61, v45\n"
    "v_exp_f32 v62, v46\n"
    "v_exp_f32 v63, v47\n"
    "v_mfma_f32_32x32x16_bf16 v[32:47], v[192:195], v[152:155], 0\n"
    "v_exp_f32 v48, v16\n"
    "v_exp_f32 v49, v17\n"
    "v_exp_f32 v50, v18\n"
    "v_exp_f32 v51, v19\n"
    "v_mfma_f32_32x32x16_bf16 v[32:47], v[196:199], v[156:159], v[32:47]\n"
    "s_waitcnt lgkmcnt(0)\n"
    "v_cmp_eq_u32 vcc, 0, v252\n"
    "v_exp_f32 v52, v20\n"
    "v_exp_f32 v53, v21\n"
    "v_exp_f32 v54, v22\n"
    "v_exp_f32 v55, v23\n"
    "v_smfmac_f32_32x32x32_bf16 v[80:95], v[204:207], v[56:63], v10\n"
    "s_cbranch_vccnz .L_SL10\n"
    ".L_BK10:\n"
    "v_exp_f32 v56, v24\n"
    "v_exp_f32 v57, v25\n"
    "v_exp_f32 v58, v26\n"
    "v_exp_f32 v59, v27\n"
    "v_smfmac_f32_32x32x32_bf16 v[96:111], v[200:203], v[48:55], v10\n"
    "ds_read_b128 v[160:163], v12 offset:0\n"
    "v_exp_f32 v60, v28\n"
    "v_exp_f32 v61, v29\n"
    "v_exp_f32 v62, v30\n"
    "v_exp_f32 v63, v31\n"
    "s_waitcnt vmcnt(0)\n"
    "v_mfma_f32_32x32x16_bf16 v[16:31], v[208:211], v[128:131], 0\n"
    "v_exp_f32 v48, v32\n"
    "v_exp_f32 v49, v33\n"
    "v_exp_f32 v50, v34\n"
    "v_exp_f32 v51, v35\n"
    "v_mfma_f32_32x32x16_bf16 v[16:31], v[212:215], v[132:135], v[16:31]\n"
    "v_exp_f32 v52, v36\n"
    "v_exp_f32 v53, v37\n"
    "v_exp_f32 v54, v38\n"
    "v_exp_f32 v55, v39\n"
    "v_smfmac_f32_32x32x32_bf16 v[96:111], v[204:207], v[56:63], v10\n"
    "v_exp_f32 v56, v40\n"
    "v_exp_f32 v57, v41\n"
    "v_exp_f32 v58, v42\n"
    "v_exp_f32 v59, v43\n"
    "v_smfmac_f32_32x32x32_bf16 v[112:127], v[200:203], v[48:55], v10\n"
    "ds_read_b128 v[164:167], v12 offset:1024\n"
    "v_exp_f32 v60, v44\n"
    "v_exp_f32 v61, v45\n"
    "v_exp_f32 v62, v46\n"
    "v_exp_f32 v63, v47\n"
    "v_mfma_f32_32x32x16_bf16 v[32:47], v[208:211], v[136:139], 0\n"
    "v_exp_f32 v48, v16\n"
    "v_exp_f32 v49, v17\n"
    "v_exp_f32 v50, v18\n"
    "v_exp_f32 v51, v19\n"
    "v_mfma_f32_32x32x16_bf16 v[32:47], v[212:215], v[140:143], v[32:47]\n"
    "v_exp_f32 v52, v20\n"
    "v_exp_f32 v53, v21\n"
    "v_exp_f32 v54, v22\n"
    "v_exp_f32 v55, v23\n"
    "v_smfmac_f32_32x32x32_bf16 v[112:127], v[204:207], v[56:63], v10\n"
    "v_exp_f32 v56, v24\n"
    "v_exp_f32 v57, v25\n"
    "v_exp_f32 v58, v26\n"
    "v_exp_f32 v59, v27\n"
    "v_smfmac_f32_32x32x32_bf16 v[64:79], v[216:219], v[48:55], v10\n"
    "ds_read_b128 v[168:171], v12 offset:2048\n"
    "v_exp_f32 v60, v28\n"
    "v_exp_f32 v61, v29\n"
    "v_exp_f32 v62, v30\n"
    "v_exp_f32 v63, v31\n"
    "v_mfma_f32_32x32x16_bf16 v[16:31], v[208:211], v[144:147], 0\n"
    "v_exp_f32 v48, v32\n"
    "v_exp_f32 v49, v33\n"
    "v_exp_f32 v50, v34\n"
    "v_exp_f32 v51, v35\n"
    "v_mfma_f32_32x32x16_bf16 v[16:31], v[212:215], v[148:151], v[16:31]\n"
    "v_exp_f32 v52, v36\n"
    "v_exp_f32 v53, v37\n"
    "v_exp_f32 v54, v38\n"
    "v_exp_f32 v55, v39\n"
    "v_smfmac_f32_32x32x32_bf16 v[64:79], v[220:223], v[56:63], v10\n"
    "v_exp_f32 v56, v40\n"
    "v_exp_f32 v57, v41\n"
    "v_exp_f32 v58, v42\n"
    "v_exp_f32 v59, v43\n"
    "v_smfmac_f32_32x32x32_bf16 v[80:95], v[216:219], v[48:55], v10\n"
    "ds_read_b128 v[172:175], v12 offset:3072\n"
    "v_exp_f32 v60, v44\n"
    "v_exp_f32 v61, v45\n"
    "v_exp_f32 v62, v46\n"
    "v_exp_f32 v63, v47\n"
    "v_mfma_f32_32x32x16_bf16 v[32:47], v[208:211], v[152:155], 0\n"
    "v_exp_f32 v48, v16\n"
    "v_exp_f32 v49, v17\n"
    "v_exp_f32 v50, v18\n"
    "v_exp_f32 v51, v19\n"
    "v_mfma_f32_32x32x16_bf16 v[32:47], v[212:215], v[156:159], v[32:47]\n"
    "v_exp_f32 v52, v20\n"
    "v_exp_f32 v53, v21\n"
    "v_exp_f32 v54, v22\n"
    "v_exp_f32 v55, v23\n"
    "v_smfmac_f32_32x32x32_bf16 v[80:95], v[220:223], v[56:63], v10\n"
    "v_exp_f32 v56, v24\n"
    "v_exp_f32 v57, v25\n"
    "v_exp_f32 v58, v26\n"
    "v_exp_f32 v59, v27\n"
    "v_smfmac_f32_32x32x32_bf16 v[96:111], v[216:219], v[48:55], v10\n"
    "ds_read_b128 v[176:179], v12 offset:4096\n"
    "v_exp_f32 v60, v28\n"
    "v_exp_f32 v61, v29\n"
    "v_exp_f32 v62, v30\n"
    "v_exp_f32 v63, v31\n"
    "s_waitcnt lgkmcnt(1)\n"
    "v_mfma_f32_32x32x16_bf16 v[16:31], v[160:163], v[128:131], 0\n"
    "v_exp_f32 v48, v32\n"
    "v_exp_f32 v49, v33\n"
    "v_exp_f32 v50, v34\n"
    "v_exp_f32 v51, v35\n"
    "v_mfma_f32_32x32x16_bf16 v[16:31], v[164:167], v[132:135], v[16:31]\n"
    "v_exp_f32 v52, v36\n"
    "v_exp_f32 v53, v37\n"
    "v_exp_f32 v54, v38\n"
    "v_exp_f32 v55, v39\n"
    "v_smfmac_f32_32x32x32_bf16 v[96:111], v[220:223], v[56:63], v10\n"
    "v_exp_f32 v56, v40\n"
    "v_exp_f32 v57, v41\n"
    "v_exp_f32 v58, v42\n"
    "v_exp_f32 v59, v43\n"
    "v_smfmac_f32_32x32x32_bf16 v[112:127], v[216:219], v[48:55], v10\n"
    "ds_read_b128 v[180:183], v12 offset:5120\n"
    "v_exp_f32 v60, v44\n"
    "v_exp_f32 v61, v45\n"
    "v_exp_f32 v62, v46\n"
    "v_exp_f32 v63, v47\n"
    "v_mfma_f32_32x32x16_bf16 v[32:47], v[160:163], v[136:139], 0\n"
    "v_exp_f32 v48, v16\n"
    "v_exp_f32 v49, v17\n"
    "v_exp_f32 v50, v18\n"
    "v_exp_f32 v51, v19\n"
    "v_mfma_f32_32x32x16_bf16 v[32:47], v[164:167], v[140:143], v[32:47]\n"
    "v_exp_f32 v52, v20\n"
    "v_exp_f32 v53, v21\n"
    "v_exp_f32 v54, v22\n"
    "v_exp_f32 v55, v23\n"
    "v_smfmac_f32_32x32x32_bf16 v[112:127], v[220:223], v[56:63], v10\n"
    "v_exp_f32 v56, v24\n"
    "v_exp_f32 v57, v25\n"
    "v_exp_f32 v58, v26\n"
    "v_exp_f32 v59, v27\n"
    "v_smfmac_f32_32x32x32_bf16 v[64:79], v[168:171], v[48:55], v10\n"
    "ds_read_b128 v[184:187], v12 offset:6144\n"
    "v_exp_f32 v60, v28\n"
    "v_exp_f32 v61, v29\n"
    "v_exp_f32 v62, v30\n"
    "v_exp_f32 v63, v31\n"
    "v_mfma_f32_32x32x16_bf16 v[16:31], v[160:163], v[144:147], 0\n"
    "v_exp_f32 v48, v32\n"
    "v_exp_f32 v49, v33\n"
    "v_exp_f32 v50, v34\n"
    "v_exp_f32 v51, v35\n"
    "v_mfma_f32_32x32x16_bf16 v[16:31], v[164:167], v[148:151], v[16:31]\n"
    "v_exp_f32 v52, v36\n"
    "v_exp_f32 v53, v37\n"
    "v_exp_f32 v54, v38\n"
    "v_exp_f32 v55, v39\n"
    "v_smfmac_f32_32x32x32_bf16 v[64:79], v[172:175], v[56:63], v10\n"
    "ds_read_b32 v252, v250 offset:176\n"
    "v_exp_f32 v56, v40\n"
    "v_exp_f32 v57, v41\n"
    "v_exp_f32 v58, v42\n"
    "v_exp_f32 v59, v43\n"
    "v_smfmac_f32_32x32x32_bf16 v[80:95], v[168:171], v[48:55], v10\n"
    "ds_read_b128 v[188:191], v12 offset:7168\n"
    "v_exp_f32 v60, v44\n"
    "v_exp_f32 v61, v45\n"
    "v_exp_f32 v62, v46\n"
    "v_exp_f32 v63, v47\n"
    "v_mfma_f32_32x32x16_bf16 v[32:47], v[160:163], v[152:155], 0\n"
    "v_exp_f32 v48, v16\n"
    "v_exp_f32 v49, v17\n"
    "v_exp_f32 v50, v18\n"
    "v_exp_f32 v51, v19\n"
    "v_mfma_f32_32x32x16_bf16 v[32:47], v[164:167], v[156:159], v[32:47]\n"
    "s_waitcnt lgkmcnt(1)\n"
    "v_cmp_eq_u32 vcc, 0, v252\n"
    "v_exp_f32 v52, v20\n"
    "v_exp_f32 v53, v21\n"
    "v_exp_f32 v54, v22\n"
    "v_exp_f32 v55, v23\n"
    "v_smfmac_f32_32x32x32_bf16 v[80:95], v[172:175], v[56:63], v10\n"
    "s_cbranch_vccnz .L_SL11\n"
    ".L_BK11:\n"
    "v_exp_f32 v56, v24\n"
    "v_exp_f32 v57, v25\n"
    "v_exp_f32 v58, v26\n"
    "v_exp_f32 v59, v27\n"
    "v_smfmac_f32_32x32x32_bf16 v[96:111], v[168:171], v[48:55], v10\n"
    "ds_read_b128 v[192:195], v12 offset:8192\n"
    "v_exp_f32 v60, v28\n"
    "v_exp_f32 v61, v29\n"
    "v_exp_f32 v62, v30\n"
    "v_exp_f32 v63, v31\n"
    "s_waitcnt lgkmcnt(1)\n"
    "v_mfma_f32_32x32x16_bf16 v[16:31], v[176:179], v[128:131], 0\n"
    "v_exp_f32 v48, v32\n"
    "v_exp_f32 v49, v33\n"
    "v_exp_f32 v50, v34\n"
    "v_exp_f32 v51, v35\n"
    "v_mfma_f32_32x32x16_bf16 v[16:31], v[180:183], v[132:135], v[16:31]\n"
    "v_exp_f32 v52, v36\n"
    "v_exp_f32 v53, v37\n"
    "v_exp_f32 v54, v38\n"
    "v_exp_f32 v55, v39\n"
    "v_smfmac_f32_32x32x32_bf16 v[96:111], v[172:175], v[56:63], v10\n"
    "v_exp_f32 v56, v40\n"
    "v_exp_f32 v57, v41\n"
    "v_exp_f32 v58, v42\n"
    "v_exp_f32 v59, v43\n"
    "v_smfmac_f32_32x32x32_bf16 v[112:127], v[168:171], v[48:55], v10\n"
    "ds_read_b128 v[196:199], v12 offset:9216\n"
    "v_exp_f32 v60, v44\n"
    "v_exp_f32 v61, v45\n"
    "v_exp_f32 v62, v46\n"
    "v_exp_f32 v63, v47\n"
    "v_mfma_f32_32x32x16_bf16 v[32:47], v[176:179], v[136:139], 0\n"
    "v_exp_f32 v48, v16\n"
    "v_exp_f32 v49, v17\n"
    "v_exp_f32 v50, v18\n"
    "v_exp_f32 v51, v19\n"
    "v_mfma_f32_32x32x16_bf16 v[32:47], v[180:183], v[140:143], v[32:47]\n"
    "v_exp_f32 v52, v20\n"
    "v_exp_f32 v53, v21\n"
    "v_exp_f32 v54, v22\n"
    "v_exp_f32 v55, v23\n"
    "v_smfmac_f32_32x32x32_bf16 v[112:127], v[172:175], v[56:63], v10\n"
    "v_exp_f32 v56, v24\n"
    "v_exp_f32 v57, v25\n"
    "v_exp_f32 v58, v26\n"
    "v_exp_f32 v59, v27\n"
    "v_smfmac_f32_32x32x32_bf16 v[64:79], v[184:187], v[48:55], v10\n"
    "ds_read_b128 v[200:203], v12 offset:10240\n"
    "v_exp_f32 v60, v28\n"
    "v_exp_f32 v61, v29\n"
    "v_exp_f32 v62, v30\n"
    "v_exp_f32 v63, v31\n"
    "v_mfma_f32_32x32x16_bf16 v[16:31], v[176:179], v[144:147], 0\n"
    "v_exp_f32 v48, v32\n"
    "v_exp_f32 v49, v33\n"
    "v_exp_f32 v50, v34\n"
    "v_exp_f32 v51, v35\n"
    "v_mfma_f32_32x32x16_bf16 v[16:31], v[180:183], v[148:151], v[16:31]\n"
    "v_exp_f32 v52, v36\n"
    "v_exp_f32 v53, v37\n"
    "v_exp_f32 v54, v38\n"
    "v_exp_f32 v55, v39\n"
    "v_smfmac_f32_32x32x32_bf16 v[64:79], v[188:191], v[56:63], v10\n"
    "v_exp_f32 v56, v40\n"
    "v_exp_f32 v57, v41\n"
    "v_exp_f32 v58, v42\n"
    "v_exp_f32 v59, v43\n"
    "v_smfmac_f32_32x32x32_bf16 v[80:95], v[184:187], v[48:55], v10\n"
    "ds_read_b128 v[204:207], v12 offset:11264\n"
    "ds_write_b32 v250, v250 offset:240\n"
    "v_exp_f32 v60, v44\n"
    "v_exp_f32 v61, v45\n"
    "v_exp_f32 v62, v46\n"
    "v_exp_f32 v63, v47\n"
    "v_mfma_f32_32x32x16_bf16 v[32:47], v[176:179], v[152:155], 0\n"
    "v_exp_f32 v48, v16\n"
    "v_exp_f32 v49, v17\n"
    "v_exp_f32 v50, v18\n"
    "v_exp_f32 v51, v19\n"
    "v_mfma_f32_32x32x16_bf16 v[32:47], v[180:183], v[156:159], v[32:47]\n"
    "v_exp_f32 v52, v20\n"
    "v_exp_f32 v53, v21\n"
    "v_exp_f32 v54, v22\n"
    "v_exp_f32 v55, v23\n"
    "v_smfmac_f32_32x32x32_bf16 v[80:95], v[188:191], v[56:63], v10\n"
    "v_exp_f32 v56, v24\n"
    "v_exp_f32 v57, v25\n"
    "v_exp_f32 v58, v26\n"
    "v_exp_f32 v59, v27\n"
    "v_smfmac_f32_32x32x32_bf16 v[96:111], v[184:187], v[48:55], v10\n"
    "ds_read_b128 v[208:211], v12 offset:12288\n"
    "v_exp_f32 v60, v28\n"
    "v_exp_f32 v61, v29\n"
    "v_exp_f32 v62, v30\n"
    "v_exp_f32 v63, v31\n"
    "s_waitcnt lgkmcnt(2)\n"
    "v_mfma_f32_32x32x16_bf16 v[16:31], v[192:195], v[128:131], 0\n"
    "v_exp_f32 v48, v32\n"
    "v_exp_f32 v49, v33\n"
    "v_exp_f32 v50, v34\n"
    "v_exp_f32 v51, v35\n"
    "v_mfma_f32_32x32x16_bf16 v[16:31], v[196:199], v[132:135], v[16:31]\n"
    "v_exp_f32 v52, v36\n"
    "v_exp_f32 v53, v37\n"
    "v_exp_f32 v54, v38\n"
    "v_exp_f32 v55, v39\n"
    "v_smfmac_f32_32x32x32_bf16 v[96:111], v[188:191], v[56:63], v10\n"
    "v_exp_f32 v56, v40\n"
    "v_exp_f32 v57, v41\n"
    "v_exp_f32 v58, v42\n"
    "v_exp_f32 v59, v43\n"
    "v_smfmac_f32_32x32x32_bf16 v[112:127], v[184:187], v[48:55], v10\n"
    "ds_read_b128 v[212:215], v12 offset:13312\n"
    "v_exp_f32 v60, v44\n"
    "v_exp_f32 v61, v45\n"
    "v_exp_f32 v62, v46\n"
    "v_exp_f32 v63, v47\n"
    "v_mfma_f32_32x32x16_bf16 v[32:47], v[192:195], v[136:139], 0\n"
    "v_exp_f32 v48, v16\n"
    "v_exp_f32 v49, v17\n"
    "v_exp_f32 v50, v18\n"
    "v_exp_f32 v51, v19\n"
    "v_mfma_f32_32x32x16_bf16 v[32:47], v[196:199], v[140:143], v[32:47]\n"
    "v_exp_f32 v52, v20\n"
    "v_exp_f32 v53, v21\n"
    "v_exp_f32 v54, v22\n"
    "v_exp_f32 v55, v23\n"
    "v_smfmac_f32_32x32x32_bf16 v[112:127], v[188:191], v[56:63], v10\n"
    "v_exp_f32 v56, v24\n"
    "v_exp_f32 v57, v25\n"
    "v_exp_f32 v58, v26\n"
    "v_exp_f32 v59, v27\n"
    "v_smfmac_f32_32x32x32_bf16 v[64:79], v[200:203], v[48:55], v10\n"
    "ds_read_b128 v[216:219], v12 offset:14336\n"
    "v_exp_f32 v60, v28\n"
    "v_exp_f32 v61, v29\n"
    "v_exp_f32 v62, v30\n"
    "v_exp_f32 v63, v31\n"
    "v_mfma_f32_32x32x16_bf16 v[16:31], v[192:195], v[144:147], 0\n"
    "v_exp_f32 v48, v32\n"
    "v_exp_f32 v49, v33\n"
    "v_exp_f32 v50, v34\n"
    "v_exp_f32 v51, v35\n"
    "v_mfma_f32_32x32x16_bf16 v[16:31], v[196:199], v[148:151], v[16:31]\n"
    "v_exp_f32 v52, v36\n"
    "v_exp_f32 v53, v37\n"
    "v_exp_f32 v54, v38\n"
    "v_exp_f32 v55, v39\n"
    "v_smfmac_f32_32x32x32_bf16 v[64:79], v[204:207], v[56:63], v10\n"
    "ds_read_b32 v252, v250 offset:192\n"
    "v_exp_f32 v56, v40\n"
    "v_exp_f32 v57, v41\n"
    "v_exp_f32 v58, v42\n"
    "v_exp_f32 v59, v43\n"
    "v_smfmac_f32_32x32x32_bf16 v[80:95], v[200:203], v[48:55], v10\n"
    "ds_read_b128 v[220:223], v12 offset:15360\n"
    "v_exp_f32 v60, v44\n"
    "v_exp_f32 v61, v45\n"
    "v_exp_f32 v62, v46\n"
    "v_exp_f32 v63, v47\n"
    "v_mfma_f32_32x32x16_bf16 v[32:47], v[192:195], v[152:155], 0\n"
    "v_exp_f32 v48, v16\n"
    "v_exp_f32 v49, v17\n"
    "v_exp_f32 v50, v18\n"
    "v_exp_f32 v51, v19\n"
    "v_mfma_f32_32x32x16_bf16 v[32:47], v[196:199], v[156:159], v[32:47]\n"
    "s_waitcnt lgkmcnt(1)\n"
    "v_cmp_eq_u32 vcc, 0, v252\n"
    "v_exp_f32 v52, v20\n"
    "v_exp_f32 v53, v21\n"
    "v_exp_f32 v54, v22\n"
    "v_exp_f32 v55, v23\n"
    "v_smfmac_f32_32x32x32_bf16 v[80:95], v[204:207], v[56:63], v10\n"
    "s_cbranch_vccnz .L_SL12\n"
    ".L_BK12:\n"
    "v_exp_f32 v56, v24\n"
    "v_exp_f32 v57, v25\n"
    "v_exp_f32 v58, v26\n"
    "v_exp_f32 v59, v27\n"
    "v_smfmac_f32_32x32x32_bf16 v[96:111], v[200:203], v[48:55], v10\n"
    "ds_read_b128 v[160:163], v12 offset:16384\n"
    "v_exp_f32 v60, v28\n"
    "v_exp_f32 v61, v29\n"
    "v_exp_f32 v62, v30\n"
    "v_exp_f32 v63, v31\n"
    "s_waitcnt lgkmcnt(1)\n"
    "v_mfma_f32_32x32x16_bf16 v[16:31], v[208:211], v[128:131], 0\n"
    "v_exp_f32 v48, v32\n"
    "v_exp_f32 v49, v33\n"
    "v_exp_f32 v50, v34\n"
    "v_exp_f32 v51, v35\n"
    "v_mfma_f32_32x32x16_bf16 v[16:31], v[212:215], v[132:135], v[16:31]\n"
    "v_exp_f32 v52, v36\n"
    "v_exp_f32 v53, v37\n"
    "v_exp_f32 v54, v38\n"
    "v_exp_f32 v55, v39\n"
    "v_smfmac_f32_32x32x32_bf16 v[96:111], v[204:207], v[56:63], v10\n"
    "v_exp_f32 v56, v40\n"
    "v_exp_f32 v57, v41\n"
    "v_exp_f32 v58, v42\n"
    "v_exp_f32 v59, v43\n"
    "v_smfmac_f32_32x32x32_bf16 v[112:127], v[200:203], v[48:55], v10\n"
    "ds_read_b128 v[164:167], v12 offset:17408\n"
    "v_exp_f32 v60, v44\n"
    "v_exp_f32 v61, v45\n"
    "v_exp_f32 v62, v46\n"
    "v_exp_f32 v63, v47\n"
    "v_mfma_f32_32x32x16_bf16 v[32:47], v[208:211], v[136:139], 0\n"
    "v_exp_f32 v48, v16\n"
    "v_exp_f32 v49, v17\n"
    "v_exp_f32 v50, v18\n"
    "v_exp_f32 v51, v19\n"
    "v_mfma_f32_32x32x16_bf16 v[32:47], v[212:215], v[140:143], v[32:47]\n"
    "v_exp_f32 v52, v20\n"
    "v_exp_f32 v53, v21\n"
    "v_exp_f32 v54, v22\n"
    "v_exp_f32 v55, v23\n"
    "v_smfmac_f32_32x32x32_bf16 v[112:127], v[204:207], v[56:63], v10\n"
    "v_exp_f32 v56, v24\n"
    "v_exp_f32 v57, v25\n"
    "v_exp_f32 v58, v26\n"
    "v_exp_f32 v59, v27\n"
    "v_smfmac_f32_32x32x32_bf16 v[64:79], v[216:219], v[48:55], v10\n"
    "ds_read_b128 v[168:171], v12 offset:18432\n"
    "v_exp_f32 v60, v28\n"
    "v_exp_f32 v61, v29\n"
    "v_exp_f32 v62, v30\n"
    "v_exp_f32 v63, v31\n"
    "v_mfma_f32_32x32x16_bf16 v[16:31], v[208:211], v[144:147], 0\n"
    "v_exp_f32 v48, v32\n"
    "v_exp_f32 v49, v33\n"
    "v_exp_f32 v50, v34\n"
    "v_exp_f32 v51, v35\n"
    "v_mfma_f32_32x32x16_bf16 v[16:31], v[212:215], v[148:151], v[16:31]\n"
    "v_exp_f32 v52, v36\n"
    "v_exp_f32 v53, v37\n"
    "v_exp_f32 v54, v38\n"
    "v_exp_f32 v55, v39\n"
    "v_smfmac_f32_32x32x32_bf16 v[64:79], v[220:223], v[56:63], v10\n"
    "v_exp_f32 v56, v40\n"
    "v_exp_f32 v57, v41\n"
    "v_exp_f32 v58, v42\n"
    "v_exp_f32 v59, v43\n"
    "v_smfmac_f32_32x32x32_bf16 v[80:95], v[216:219], v[48:55], v10\n"
    "ds_read_b128 v[172:175], v12 offset:19456\n"
    "ds_write_b32 v250, v250 offset:256\n"
    "v_exp_f32 v60, v44\n"
    "v_exp_f32 v61, v45\n"
    "v_exp_f32 v62, v46\n"
    "v_exp_f32 v63, v47\n"
    "v_mfma_f32_32x32x16_bf16 v[32:47], v[208:211], v[152:155], 0\n"
    "v_exp_f32 v48, v16\n"
    "v_exp_f32 v49, v17\n"
    "v_exp_f32 v50, v18\n"
    "v_exp_f32 v51, v19\n"
    "v_mfma_f32_32x32x16_bf16 v[32:47], v[212:215], v[156:159], v[32:47]\n"
    "v_exp_f32 v52, v20\n"
    "v_exp_f32 v53, v21\n"
    "v_exp_f32 v54, v22\n"
    "v_exp_f32 v55, v23\n"
    "v_smfmac_f32_32x32x32_bf16 v[80:95], v[220:223], v[56:63], v10\n"
    "v_exp_f32 v56, v24\n"
    "v_exp_f32 v57, v25\n"
    "v_exp_f32 v58, v26\n"
    "v_exp_f32 v59, v27\n"
    "v_smfmac_f32_32x32x32_bf16 v[96:111], v[216:219], v[48:55], v10\n"
    "ds_read_b128 v[176:179], v12 offset:20480\n"
    "v_exp_f32 v60, v28\n"
    "v_exp_f32 v61, v29\n"
    "v_exp_f32 v62, v30\n"
    "v_exp_f32 v63, v31\n"
    "s_waitcnt lgkmcnt(2)\n"
    "v_mfma_f32_32x32x16_bf16 v[16:31], v[160:163], v[128:131], 0\n"
    "v_exp_f32 v48, v32\n"
    "v_exp_f32 v49, v33\n"
    "v_exp_f32 v50, v34\n"
    "v_exp_f32 v51, v35\n"
    "v_mfma_f32_32x32x16_bf16 v[16:31], v[164:167], v[132:135], v[16:31]\n"
    "v_exp_f32 v52, v36\n"
    "v_exp_f32 v53, v37\n"
    "v_exp_f32 v54, v38\n"
    "v_exp_f32 v55, v39\n"
    "v_smfmac_f32_32x32x32_bf16 v[96:111], v[220:223], v[56:63], v10\n"
    "v_exp_f32 v56, v40\n"
    "v_exp_f32 v57, v41\n"
    "v_exp_f32 v58, v42\n"
    "v_exp_f32 v59, v43\n"
    "v_smfmac_f32_32x32x32_bf16 v[112:127], v[216:219], v[48:55], v10\n"
    "ds_read_b128 v[180:183], v12 offset:21504\n"
    "v_exp_f32 v60, v44\n"
    "v_exp_f32 v61, v45\n"
    "v_exp_f32 v62, v46\n"
    "v_exp_f32 v63, v47\n"
    "v_mfma_f32_32x32x16_bf16 v[32:47], v[160:163], v[136:139], 0\n"
    "v_exp_f32 v48, v16\n"
    "v_exp_f32 v49, v17\n"
    "v_exp_f32 v50, v18\n"
    "v_exp_f32 v51, v19\n"
    "v_mfma_f32_32x32x16_bf16 v[32:47], v[164:167], v[140:143], v[32:47]\n"
    "v_exp_f32 v52, v20\n"
    "v_exp_f32 v53, v21\n"
    "v_exp_f32 v54, v22\n"
    "v_exp_f32 v55, v23\n"
    "v_smfmac_f32_32x32x32_bf16 v[112:127], v[220:223], v[56:63], v10\n"
    "v_exp_f32 v56, v24\n"
    "v_exp_f32 v57, v25\n"
    "v_exp_f32 v58, v26\n"
    "v_exp_f32 v59, v27\n"
    "v_smfmac_f32_32x32x32_bf16 v[64:79], v[168:171], v[48:55], v10\n"
    "ds_read_b128 v[184:187], v12 offset:22528\n"
    "v_exp_f32 v60, v28\n"
    "v_exp_f32 v61, v29\n"
    "v_exp_f32 v62, v30\n"
    "v_exp_f32 v63, v31\n"
    "v_mfma_f32_32x32x16_bf16 v[16:31], v[160:163], v[144:147], 0\n"
    "v_exp_f32 v48, v32\n"
    "v_exp_f32 v49, v33\n"
    "v_exp_f32 v50, v34\n"
    "v_exp_f32 v51, v35\n"
    "v_mfma_f32_32x32x16_bf16 v[16:31], v[164:167], v[148:151], v[16:31]\n"
    "v_exp_f32 v52, v36\n"
    "v_exp_f32 v53, v37\n"
    "v_exp_f32 v54, v38\n"
    "v_exp_f32 v55, v39\n"
    "v_smfmac_f32_32x32x32_bf16 v[64:79], v[172:175], v[56:63], v10\n"
    "v_exp_f32 v56, v40\n"
    "v_exp_f32 v57, v41\n"
    "v_exp_f32 v58, v42\n"
    "v_exp_f32 v59, v43\n"
    "v_smfmac_f32_32x32x32_bf16 v[80:95], v[168:171], v[48:55], v10\n"
    "ds_read_b128 v[188:191], v12 offset:23552\n"
    "v_exp_f32 v60, v44\n"
    "v_exp_f32 v61, v45\n"
    "v_exp_f32 v62, v46\n"
    "v_exp_f32 v63, v47\n"
    "v_mfma_f32_32x32x16_bf16 v[32:47], v[160:163], v[152:155], 0\n"
    "v_exp_f32 v48, v16\n"
    "v_exp_f32 v49, v17\n"
    "v_exp_f32 v50, v18\n"
    "v_exp_f32 v51, v19\n"
    "v_mfma_f32_32x32x16_bf16 v[32:47], v[164:167], v[156:159], v[32:47]\n"
    "v_exp_f32 v52, v20\n"
    "v_exp_f32 v53, v21\n"
    "v_exp_f32 v54, v22\n"
    "v_exp_f32 v55, v23\n"
    "v_smfmac_f32_32x32x32_bf16 v[80:95], v[172:175], v[56:63], v10\n"
    "v_exp_f32 v56, v24\n"
    "v_exp_f32 v57, v25\n"
    "v_exp_f32 v58, v26\n"
    "v_exp_f32 v59, v27\n"
    "v_smfmac_f32_32x32x32_bf16 v[96:111], v[168:171], v[48:55], v10\n"
    "ds_read_b128 v[192:195], v12 offset:24576\n"
    "v_exp_f32 v60, v28\n"
    "v_exp_f32 v61, v29\n"
    "v_exp_f32 v62, v30\n"
    "v_exp_f32 v63, v31\n"
    "s_waitcnt lgkmcnt(1)\n"
    "v_mfma_f32_32x32x16_bf16 v[16:31], v[176:179], v[128:131], 0\n"
    "v_exp_f32 v48, v32\n"
    "v_exp_f32 v49, v33\n"
    "v_exp_f32 v50, v34\n"
    "v_exp_f32 v51, v35\n"
    "v_mfma_f32_32x32x16_bf16 v[16:31], v[180:183], v[132:135], v[16:31]\n"
    "v_exp_f32 v52, v36\n"
    "v_exp_f32 v53, v37\n"
    "v_exp_f32 v54, v38\n"
    "v_exp_f32 v55, v39\n"
    "v_smfmac_f32_32x32x32_bf16 v[96:111], v[172:175], v[56:63], v10\n"
    "v_exp_f32 v56, v40\n"
    "v_exp_f32 v57, v41\n"
    "v_exp_f32 v58, v42\n"
    "v_exp_f32 v59, v43\n"
    "v_smfmac_f32_32x32x32_bf16 v[112:127], v[168:171], v[48:55], v10\n"
    "ds_read_b128 v[196:199], v12 offset:25600\n"
    "v_exp_f32 v60, v44\n"
    "v_exp_f32 v61, v45\n"
    "v_exp_f32 v62, v46\n"
    "v_exp_f32 v63, v47\n"
    "v_mfma_f32_32x32x16_bf16 v[32:47], v[176:179], v[136:139], 0\n"
    "v_exp_f32 v48, v16\n"
    "v_exp_f32 v49, v17\n"
    "v_exp_f32 v50, v18\n"
    "v_exp_f32 v51, v19\n"
    "v_mfma_f32_32x32x16_bf16 v[32:47], v[180:183], v[140:143], v[32:47]\n"
    "v_exp_f32 v52, v20\n"
    "v_exp_f32 v53, v21\n"
    "v_exp_f32 v54, v22\n"
    "v_exp_f32 v55, v23\n"
    "v_smfmac_f32_32x32x32_bf16 v[112:127], v[172:175], v[56:63], v10\n"
    "v_exp_f32 v56, v24\n"
    "v_exp_f32 v57, v25\n"
    "v_exp_f32 v58, v26\n"
    "v_exp_f32 v59, v27\n"
    "v_smfmac_f32_32x32x32_bf16 v[64:79], v[184:187], v[48:55], v10\n"
    "ds_read_b128 v[200:203], v12 offset:26624\n"
    "v_exp_f32 v60, v28\n"
    "v_exp_f32 v61, v29\n"
    "v_exp_f32 v62, v30\n"
    "v_exp_f32 v63, v31\n"
    "v_mfma_f32_32x32x16_bf16 v[16:31], v[176:179], v[144:147], 0\n"
    "v_exp_f32 v48, v32\n"
    "v_exp_f32 v49, v33\n"
    "v_exp_f32 v50, v34\n"
    "v_exp_f32 v51, v35\n"
    "v_mfma_f32_32x32x16_bf16 v[16:31], v[180:183], v[148:151], v[16:31]\n"
    "v_exp_f32 v52, v36\n"
    "v_exp_f32 v53, v37\n"
    "v_exp_f32 v54, v38\n"
    "v_exp_f32 v55, v39\n"
    "v_smfmac_f32_32x32x32_bf16 v[64:79], v[188:191], v[56:63], v10\n"
    "ds_read_b32 v252, v250 offset:208\n"
    "v_exp_f32 v56, v40\n"
    "v_exp_f32 v57, v41\n"
    "v_exp_f32 v58, v42\n"
    "v_exp_f32 v59, v43\n"
    "v_smfmac_f32_32x32x32_bf16 v[80:95], v[184:187], v[48:55], v10\n"
    "ds_read_b128 v[204:207], v12 offset:27648\n"
    "v_exp_f32 v60, v44\n"
    "v_exp_f32 v61, v45\n"
    "v_exp_f32 v62, v46\n"
    "v_exp_f32 v63, v47\n"
    "v_mfma_f32_32x32x16_bf16 v[32:47], v[176:179], v[152:155], 0\n"
    "v_exp_f32 v48, v16\n"
    "v_exp_f32 v49, v17\n"
    "v_exp_f32 v50, v18\n"
    "v_exp_f32 v51, v19\n"
    "v_mfma_f32_32x32x16_bf16 v[32:47], v[180:183], v[156:159], v[32:47]\n"
    "s_waitcnt lgkmcnt(1)\n"
    "v_cmp_eq_u32 vcc, 0, v252\n"
    "v_exp_f32 v52, v20\n"
    "v_exp_f32 v53, v21\n"
    "v_exp_f32 v54, v22\n"
    "v_exp_f32 v55, v23\n"
    "v_smfmac_f32_32x32x32_bf16 v[80:95], v[188:191], v[56:63], v10\n"
    "s_cbranch_vccnz .L_SL13\n"
    ".L_BK13:\n"
    "v_exp_f32 v56, v24\n"
    "v_exp_f32 v57, v25\n"
    "v_exp_f32 v58, v26\n"
    "v_exp_f32 v59, v27\n"
    "v_smfmac_f32_32x32x32_bf16 v[96:111], v[184:187], v[48:55], v10\n"
    "ds_read_b128 v[208:211], v12 offset:0\n"
    "v_exp_f32 v60, v28\n"
    "v_exp_f32 v61, v29\n"
    "v_exp_f32 v62, v30\n"
    "v_exp_f32 v63, v31\n"
    "s_waitcnt lgkmcnt(1)\n"
    "v_mfma_f32_32x32x16_bf16 v[16:31], v[192:195], v[128:131], 0\n"
    "v_exp_f32 v48, v32\n"
    "v_exp_f32 v49, v33\n"
    "v_exp_f32 v50, v34\n"
    "v_exp_f32 v51, v35\n"
    "v_mfma_f32_32x32x16_bf16 v[16:31], v[196:199], v[132:135], v[16:31]\n"
    "v_exp_f32 v52, v36\n"
    "v_exp_f32 v53, v37\n"
    "v_exp_f32 v54, v38\n"
    "v_exp_f32 v55, v39\n"
    "v_smfmac_f32_32x32x32_bf16 v[96:111], v[188:191], v[56:63], v10\n"
    "v_exp_f32 v56, v40\n"
    "v_exp_f32 v57, v41\n"
    "v_exp_f32 v58, v42\n"
    "v_exp_f32 v59, v43\n"
    "v_smfmac_f32_32x32x32_bf16 v[112:127], v[184:187], v[48:55], v10\n"
    "ds_read_b128 v[212:215], v12 offset:1024\n"
    "v_exp_f32 v60, v44\n"
    "v_exp_f32 v61, v45\n"
    "v_exp_f32 v62, v46\n"
    "v_exp_f32 v63, v47\n"
    "v_mfma_f32_32x32x16_bf16 v[32:47], v[192:195], v[136:139], 0\n"
    "v_exp_f32 v48, v16\n"
    "v_exp_f32 v49, v17\n"
    "v_exp_f32 v50, v18\n"
    "v_exp_f32 v51, v19\n"
    "v_mfma_f32_32x32x16_bf16 v[32:47], v[196:199], v[140:143], v[32:47]\n"
    "v_exp_f32 v52, v20\n"
    "v_exp_f32 v53, v21\n"
    "v_exp_f32 v54, v22\n"
    "v_exp_f32 v55, v23\n"
    "v_smfmac_f32_32x32x32_bf16 v[112:127], v[188:191], v[56:63], v10\n"
    "v_exp_f32 v56, v24\n"
    "v_exp_f32 v57, v25\n"
    "v_exp_f32 v58, v26\n"
    "v_exp_f32 v59, v27\n"
    "v_smfmac_f32_32x32x32_bf16 v[64:79], v[200:203], v[48:55], v10\n"
    "ds_read_b128 v[216:219], v12 offset:2048\n"
    "v_exp_f32 v60, v28\n"
    "v_exp_f32 v61, v29\n"
    "v_exp_f32 v62, v30\n"
    "v_exp_f32 v63, v31\n"
    "v_mfma_f32_32x32x16_bf16 v[16:31], v[192:195], v[144:147], 0\n"
    "v_exp_f32 v48, v32\n"
    "v_exp_f32 v49, v33\n"
    "v_exp_f32 v50, v34\n"
    "v_exp_f32 v51, v35\n"
    "v_mfma_f32_32x32x16_bf16 v[16:31], v[196:199], v[148:151], v[16:31]\n"
    "v_exp_f32 v52, v36\n"
    "v_exp_f32 v53, v37\n"
    "v_exp_f32 v54, v38\n"
    "v_exp_f32 v55, v39\n"
    "v_smfmac_f32_32x32x32_bf16 v[64:79], v[204:207], v[56:63], v10\n"
    "v_exp_f32 v56, v40\n"
    "v_exp_f32 v57, v41\n"
    "v_exp_f32 v58, v42\n"
    "v_exp_f32 v59, v43\n"
    "v_smfmac_f32_32x32x32_bf16 v[80:95], v[200:203], v[48:55], v10\n"
    "ds_read_b128 v[220:223], v12 offset:3072\n"
    "v_exp_f32 v60, v44\n"
    "v_exp_f32 v61, v45\n"
    "v_exp_f32 v62, v46\n"
    "v_exp_f32 v63, v47\n"
    "v_mfma_f32_32x32x16_bf16 v[32:47], v[192:195], v[152:155], 0\n"
    "v_exp_f32 v48, v16\n"
    "v_exp_f32 v49, v17\n"
    "v_exp_f32 v50, v18\n"
    "v_exp_f32 v51, v19\n"
    "v_mfma_f32_32x32x16_bf16 v[32:47], v[196:199], v[156:159], v[32:47]\n"
    "v_exp_f32 v52, v20\n"
    "v_exp_f32 v53, v21\n"
    "v_exp_f32 v54, v22\n"
    "v_exp_f32 v55, v23\n"
    "v_smfmac_f32_32x32x32_bf16 v[80:95], v[204:207], v[56:63], v10\n"
    "v_exp_f32 v56, v24\n"
    "v_exp_f32 v57, v25\n"
    "v_exp_f32 v58, v26\n"
    "v_exp_f32 v59, v27\n"
    "v_smfmac_f32_32x32x32_bf16 v[96:111], v[200:203], v[48:55], v10\n"
    "ds_read_b128 v[160:163], v12 offset:4096\n"
    "v_exp_f32 v60, v28\n"
    "v_exp_f32 v61, v29\n"
    "v_exp_f32 v62, v30\n"
    "v_exp_f32 v63, v31\n"
    "s_waitcnt lgkmcnt(1)\n"
    "v_mfma_f32_32x32x16_bf16 v[16:31], v[208:211], v[128:131], 0\n"
    "v_exp_f32 v48, v32\n"
    "v_exp_f32 v49, v33\n"
    "v_exp_f32 v50, v34\n"
    "v_exp_f32 v51, v35\n"
    "v_mfma_f32_32x32x16_bf16 v[16:31], v[212:215], v[132:135], v[16:31]\n"
    "v_exp_f32 v52, v36\n"
    "v_exp_f32 v53, v37\n"
    "v_exp_f32 v54, v38\n"
    "v_exp_f32 v55, v39\n"
    "v_smfmac_f32_32x32x32_bf16 v[96:111], v[204:207], v[56:63], v10\n"
    "v_exp_f32 v56, v40\n"
    "v_exp_f32 v57, v41\n"
    "v_exp_f32 v58, v42\n"
    "v_exp_f32 v59, v43\n"
    "v_smfmac_f32_32x32x32_bf16 v[112:127], v[200:203], v[48:55], v10\n"
    "ds_read_b128 v[164:167], v12 offset:5120\n"
    "v_exp_f32 v60, v44\n"
    "v_exp_f32 v61, v45\n"
    "v_exp_f32 v62, v46\n"
    "v_exp_f32 v63, v47\n"
    "v_mfma_f32_32x32x16_bf16 v[32:47], v[208:211], v[136:139], 0\n"
    "v_exp_f32 v48, v16\n"
    "v_exp_f32 v49, v17\n"
    "v_exp_f32 v50, v18\n"
    "v_exp_f32 v51, v19\n"
    "v_mfma_f32_32x32x16_bf16 v[32:47], v[212:215], v[140:143], v[32:47]\n"
    "v_exp_f32 v52, v20\n"
    "v_exp_f32 v53, v21\n"
    "v_exp_f32 v54, v22\n"
    "v_exp_f32 v55, v23\n"
    "v_smfmac_f32_32x32x32_bf16 v[112:127], v[204:207], v[56:63], v10\n"
    "v_exp_f32 v56, v24\n"
    "v_exp_f32 v57, v25\n"
    "v_exp_f32 v58, v26\n"
    "v_exp_f32 v59, v27\n"
    "v_smfmac_f32_32x32x32_bf16 v[64:79], v[216:219], v[48:55], v10\n"
    "ds_read_b128 v[168:171], v12 offset:6144\n"
    "v_exp_f32 v60, v28\n"
    "v_exp_f32 v61, v29\n"
    "v_exp_f32 v62, v30\n"
    "v_exp_f32 v63, v31\n"
    "v_mfma_f32_32x32x16_bf16 v[16:31], v[208:211], v[144:147], 0\n"
    "v_exp_f32 v48, v32\n"
    "v_exp_f32 v49, v33\n"
    "v_exp_f32 v50, v34\n"
    "v_exp_f32 v51, v35\n"
    "v_mfma_f32_32x32x16_bf16 v[16:31], v[212:215], v[148:151], v[16:31]\n"
    "v_exp_f32 v52, v36\n"
    "v_exp_f32 v53, v37\n"
    "v_exp_f32 v54, v38\n"
    "v_exp_f32 v55, v39\n"
    "v_smfmac_f32_32x32x32_bf16 v[64:79], v[220:223], v[56:63], v10\n"
    "v_exp_f32 v56, v40\n"
    "v_exp_f32 v57, v41\n"
    "v_exp_f32 v58, v42\n"
    "v_exp_f32 v59, v43\n"
    "v_smfmac_f32_32x32x32_bf16 v[80:95], v[216:219], v[48:55], v10\n"
    "ds_read_b128 v[172:175], v12 offset:7168\n"
    "v_exp_f32 v60, v44\n"
    "v_exp_f32 v61, v45\n"
    "v_exp_f32 v62, v46\n"
    "v_exp_f32 v63, v47\n"
    "v_mfma_f32_32x32x16_bf16 v[32:47], v[208:211], v[152:155], 0\n"
    "v_exp_f32 v48, v16\n"
    "v_exp_f32 v49, v17\n"
    "v_exp_f32 v50, v18\n"
    "v_exp_f32 v51, v19\n"
    "v_mfma_f32_32x32x16_bf16 v[32:47], v[212:215], v[156:159], v[32:47]\n"
    "v_exp_f32 v52, v20\n"
    "v_exp_f32 v53, v21\n"
    "v_exp_f32 v54, v22\n"
    "v_exp_f32 v55, v23\n"
    "v_smfmac_f32_32x32x32_bf16 v[80:95], v[220:223], v[56:63], v10\n"
    "v_exp_f32 v56, v24\n"
    "v_exp_f32 v57, v25\n"
    "v_exp_f32 v58, v26\n"
    "v_exp_f32 v59, v27\n"
    "v_smfmac_f32_32x32x32_bf16 v[96:111], v[216:219], v[48:55], v10\n"
    "ds_read_b128 v[176:179], v12 offset:8192\n"
    "v_exp_f32 v60, v28\n"
    "v_exp_f32 v61, v29\n"
    "v_exp_f32 v62, v30\n"
    "v_exp_f32 v63, v31\n"
    "s_waitcnt lgkmcnt(1)\n"
    "v_mfma_f32_32x32x16_bf16 v[16:31], v[160:163], v[128:131], 0\n"
    "v_exp_f32 v48, v32\n"
    "v_exp_f32 v49, v33\n"
    "v_exp_f32 v50, v34\n"
    "v_exp_f32 v51, v35\n"
    "v_mfma_f32_32x32x16_bf16 v[16:31], v[164:167], v[132:135], v[16:31]\n"
    "v_exp_f32 v52, v36\n"
    "v_exp_f32 v53, v37\n"
    "v_exp_f32 v54, v38\n"
    "v_exp_f32 v55, v39\n"
    "v_smfmac_f32_32x32x32_bf16 v[96:111], v[220:223], v[56:63], v10\n"
    "v_exp_f32 v56, v40\n"
    "v_exp_f32 v57, v41\n"
    "v_exp_f32 v58, v42\n"
    "v_exp_f32 v59, v43\n"
    "v_smfmac_f32_32x32x32_bf16 v[112:127], v[216:219], v[48:55], v10\n"
    "ds_read_b128 v[180:183], v12 offset:9216\n"
    "v_exp_f32 v60, v44\n"
    "v_exp_f32 v61, v45\n"
    "v_exp_f32 v62, v46\n"
    "v_exp_f32 v63, v47\n"
    "v_mfma_f32_32x32x16_bf16 v[32:47], v[160:163], v[136:139], 0\n"
    "v_exp_f32 v48, v16\n"
    "v_exp_f32 v49, v17\n"
    "v_exp_f32 v50, v18\n"
    "v_exp_f32 v51, v19\n"
    "v_mfma_f32_32x32x16_bf16 v[32:47], v[164:167], v[140:143], v[32:47]\n"
    "v_exp_f32 v52, v20\n"
    "v_exp_f32 v53, v21\n"
    "v_exp_f32 v54, v22\n"
    "v_exp_f32 v55, v23\n"
    "v_smfmac_f32_32x32x32_bf16 v[112:127], v[220:223], v[56:63], v10\n"
    "v_exp_f32 v56, v24\n"
    "v_exp_f32 v57, v25\n"
    "v_exp_f32 v58, v26\n"
    "v_exp_f32 v59, v27\n"
    "v_smfmac_f32_32x32x32_bf16 v[64:79], v[168:171], v[48:55], v10\n"
    "ds_read_b128 v[184:187], v12 offset:10240\n"
    "v_exp_f32 v60, v28\n"
    "v_exp_f32 v61, v29\n"
    "v_exp_f32 v62, v30\n"
    "v_exp_f32 v63, v31\n"
    "v_mfma_f32_32x32x16_bf16 v[16:31], v[160:163], v[144:147], 0\n"
    "v_exp_f32 v48, v32\n"
    "v_exp_f32 v49, v33\n"
    "v_exp_f32 v50, v34\n"
    "v_exp_f32 v51, v35\n"
    "v_mfma_f32_32x32x16_bf16 v[16:31], v[164:167], v[148:151], v[16:31]\n"
    "v_exp_f32 v52, v36\n"
    "v_exp_f32 v53, v37\n"
    "v_exp_f32 v54, v38\n"
    "v_exp_f32 v55, v39\n"
    "v_smfmac_f32_32x32x32_bf16 v[64:79], v[172:175], v[56:63], v10\n"
    "ds_read_b32 v252, v250 offset:224\n"
    "v_exp_f32 v56, v40\n"
    "v_exp_f32 v57, v41\n"
    "v_exp_f32 v58, v42\n"
    "v_exp_f32 v59, v43\n"
    "v_smfmac_f32_32x32x32_bf16 v[80:95], v[168:171], v[48:55], v10\n"
    "ds_read_b128 v[188:191], v12 offset:11264\n"
    "v_exp_f32 v60, v44\n"
    "v_exp_f32 v61, v45\n"
    "v_exp_f32 v62, v46\n"
    "v_exp_f32 v63, v47\n"
    "v_mfma_f32_32x32x16_bf16 v[32:47], v[160:163], v[152:155], 0\n"
    "v_exp_f32 v48, v16\n"
    "v_exp_f32 v49, v17\n"
    "v_exp_f32 v50, v18\n"
    "v_exp_f32 v51, v19\n"
    "v_mfma_f32_32x32x16_bf16 v[32:47], v[164:167], v[156:159], v[32:47]\n"
    "s_waitcnt lgkmcnt(1)\n"
    "v_cmp_eq_u32 vcc, 0, v252\n"
    "v_exp_f32 v52, v20\n"
    "v_exp_f32 v53, v21\n"
    "v_exp_f32 v54, v22\n"
    "v_exp_f32 v55, v23\n"
    "v_smfmac_f32_32x32x32_bf16 v[80:95], v[172:175], v[56:63], v10\n"
    "s_cbranch_vccnz .L_SL14\n"
    ".L_BK14:\n"
    "v_exp_f32 v56, v24\n"
    "v_exp_f32 v57, v25\n"
    "v_exp_f32 v58, v26\n"
    "v_exp_f32 v59, v27\n"
    "v_smfmac_f32_32x32x32_bf16 v[96:111], v[168:171], v[48:55], v10\n"
    "ds_read_b128 v[192:195], v12 offset:12288\n"
    "v_exp_f32 v60, v28\n"
    "v_exp_f32 v61, v29\n"
    "v_exp_f32 v62, v30\n"
    "v_exp_f32 v63, v31\n"
    "s_waitcnt lgkmcnt(1)\n"
    "v_mfma_f32_32x32x16_bf16 v[16:31], v[176:179], v[128:131], 0\n"
    "v_exp_f32 v48, v32\n"
    "v_exp_f32 v49, v33\n"
    "v_exp_f32 v50, v34\n"
    "v_exp_f32 v51, v35\n"
    "v_mfma_f32_32x32x16_bf16 v[16:31], v[180:183], v[132:135], v[16:31]\n"
    "v_exp_f32 v52, v36\n"
    "v_exp_f32 v53, v37\n"
    "v_exp_f32 v54, v38\n"
    "v_exp_f32 v55, v39\n"
    "v_smfmac_f32_32x32x32_bf16 v[96:111], v[172:175], v[56:63], v10\n"
    "v_exp_f32 v56, v40\n"
    "v_exp_f32 v57, v41\n"
    "v_exp_f32 v58, v42\n"
    "v_exp_f32 v59, v43\n"
    "v_smfmac_f32_32x32x32_bf16 v[112:127], v[168:171], v[48:55], v10\n"
    "ds_read_b128 v[196:199], v12 offset:13312\n"
    "v_exp_f32 v60, v44\n"
    "v_exp_f32 v61, v45\n"
    "v_exp_f32 v62, v46\n"
    "v_exp_f32 v63, v47\n"
    "v_mfma_f32_32x32x16_bf16 v[32:47], v[176:179], v[136:139], 0\n"
    "v_exp_f32 v48, v16\n"
    "v_exp_f32 v49, v17\n"
    "v_exp_f32 v50, v18\n"
    "v_exp_f32 v51, v19\n"
    "v_mfma_f32_32x32x16_bf16 v[32:47], v[180:183], v[140:143], v[32:47]\n"
    "v_exp_f32 v52, v20\n"
    "v_exp_f32 v53, v21\n"
    "v_exp_f32 v54, v22\n"
    "v_exp_f32 v55, v23\n"
    "v_smfmac_f32_32x32x32_bf16 v[112:127], v[172:175], v[56:63], v10\n"
    "v_exp_f32 v56, v24\n"
    "v_exp_f32 v57, v25\n"
    "v_exp_f32 v58, v26\n"
    "v_exp_f32 v59, v27\n"
    "v_smfmac_f32_32x32x32_bf16 v[64:79], v[184:187], v[48:55], v10\n"
    "ds_read_b128 v[200:203], v12 offset:14336\n"
    "v_exp_f32 v60, v28\n"
    "v_exp_f32 v61, v29\n"
    "v_exp_f32 v62, v30\n"
    "v_exp_f32 v63, v31\n"
    "v_mfma_f32_32x32x16_bf16 v[16:31], v[176:179], v[144:147], 0\n"
    "v_exp_f32 v48, v32\n"
    "v_exp_f32 v49, v33\n"
    "v_exp_f32 v50, v34\n"
    "v_exp_f32 v51, v35\n"
    "v_mfma_f32_32x32x16_bf16 v[16:31], v[180:183], v[148:151], v[16:31]\n"
    "v_exp_f32 v52, v36\n"
    "v_exp_f32 v53, v37\n"
    "v_exp_f32 v54, v38\n"
    "v_exp_f32 v55, v39\n"
    "v_smfmac_f32_32x32x32_bf16 v[64:79], v[188:191], v[56:63], v10\n"
    "v_exp_f32 v56, v40\n"
    "v_exp_f32 v57, v41\n"
    "v_exp_f32 v58, v42\n"
    "v_exp_f32 v59, v43\n"
    "v_smfmac_f32_32x32x32_bf16 v[80:95], v[184:187], v[48:55], v10\n"
    "ds_read_b128 v[204:207], v12 offset:15360\n"
    "v_exp_f32 v60, v44\n"
    "v_exp_f32 v61, v45\n"
    "v_exp_f32 v62, v46\n"
    "v_exp_f32 v63, v47\n"
    "v_mfma_f32_32x32x16_bf16 v[32:47], v[176:179], v[152:155], 0\n"
    "v_exp_f32 v48, v16\n"
    "v_exp_f32 v49, v17\n"
    "v_exp_f32 v50, v18\n"
    "v_exp_f32 v51, v19\n"
    "v_mfma_f32_32x32x16_bf16 v[32:47], v[180:183], v[156:159], v[32:47]\n"
    "ds_read_b128 v[208:211], v12 offset:16384\n"
    "v_exp_f32 v52, v20\n"
    "v_exp_f32 v53, v21\n"
    "v_exp_f32 v54, v22\n"
    "v_exp_f32 v55, v23\n"
    "v_smfmac_f32_32x32x32_bf16 v[80:95], v[188:191], v[56:63], v10\n"
    "v_exp_f32 v56, v24\n"
    "v_exp_f32 v57, v25\n"
    "v_exp_f32 v58, v26\n"
    "v_exp_f32 v59, v27\n"
    "v_smfmac_f32_32x32x32_bf16 v[96:111], v[184:187], v[48:55], v10\n"
    "ds_read_b128 v[212:215], v12 offset:17408\n"
    "v_exp_f32 v60, v28\n"
    "v_exp_f32 v61, v29\n"
    "v_exp_f32 v62, v30\n"
    "v_exp_f32 v63, v31\n"
    "s_waitcnt lgkmcnt(2)\n"
    "v_mfma_f32_32x32x16_bf16 v[16:31], v[192:195], v[128:131], 0\n"
    "v_exp_f32 v48, v32\n"
    "v_exp_f32 v49, v33\n"
    "v_exp_f32 v50, v34\n"
    "v_exp_f32 v51, v35\n"
    "v_mfma_f32_32x32x16_bf16 v[16:31], v[196:199], v[132:135], v[16:31]\n"
    "ds_read_b128 v[216:219], v12 offset:18432\n"
    "v_exp_f32 v52, v36\n"
    "v_exp_f32 v53, v37\n"
    "v_exp_f32 v54, v38\n"
    "v_exp_f32 v55, v39\n"
    "v_smfmac_f32_32x32x32_bf16 v[96:111], v[188:191], v[56:63], v10\n"
    "v_exp_f32 v56, v40\n"
    "v_exp_f32 v57, v41\n"
    "v_exp_f32 v58, v42\n"
    "v_exp_f32 v59, v43\n"
    "v_smfmac_f32_32x32x32_bf16 v[112:127], v[184:187], v[48:55], v10\n"
    "ds_read_b128 v[220:223], v12 offset:19456\n"
    "v_exp_f32 v60, v44\n"
    "v_exp_f32 v61, v45\n"
    "v_exp_f32 v62, v46\n"
    "v_exp_f32 v63, v47\n"
    "s_waitcnt lgkmcnt(0)\n"
    "v_mfma_f32_32x32x16_bf16 v[32:47], v[208:211], v[128:131], 0\n"
    "v_exp_f32 v48, v16\n"
    "v_exp_f32 v49, v17\n"
    "v_exp_f32 v50, v18\n"
    "v_exp_f32 v51, v19\n"
    "v_mfma_f32_32x32x16_bf16 v[32:47], v[212:215], v[132:135], v[32:47]\n"
    "v_exp_f32 v52, v20\n"
    "v_exp_f32 v53, v21\n"
    "v_exp_f32 v54, v22\n"
    "v_exp_f32 v55, v23\n"
    "v_smfmac_f32_32x32x32_bf16 v[112:127], v[188:191], v[56:63], v10\n"
    "v_exp_f32 v56, v24\n"
    "v_exp_f32 v57, v25\n"
    "v_exp_f32 v58, v26\n"
    "v_exp_f32 v59, v27\n"
    "v_smfmac_f32_32x32x32_bf16 v[64:79], v[200:203], v[48:55], v10\n"
    "v_exp_f32 v60, v28\n"
    "v_exp_f32 v61, v29\n"
    "v_exp_f32 v62, v30\n"
    "v_exp_f32 v63, v31\n"
    "v_mfma_f32_32x32x16_bf16 v[16:31], v[192:195], v[136:139], 0\n"
    "v_exp_f32 v48, v32\n"
    "v_exp_f32 v49, v33\n"
    "v_exp_f32 v50, v34\n"
    "v_exp_f32 v51, v35\n"
    "v_mfma_f32_32x32x16_bf16 v[16:31], v[196:199], v[140:143], v[16:31]\n"
    "v_exp_f32 v52, v36\n"
    "v_exp_f32 v53, v37\n"
    "v_exp_f32 v54, v38\n"
    "v_exp_f32 v55, v39\n"
    "v_smfmac_f32_32x32x32_bf16 v[64:79], v[204:207], v[56:63], v10\n"
    "v_exp_f32 v56, v40\n"
    "v_exp_f32 v57, v41\n"
    "v_exp_f32 v58, v42\n"
    "v_exp_f32 v59, v43\n"
    "v_smfmac_f32_32x32x32_bf16 v[64:79], v[216:219], v[48:55], v10\n"
    "v_exp_f32 v60, v44\n"
    "v_exp_f32 v61, v45\n"
    "v_exp_f32 v62, v46\n"
    "v_exp_f32 v63, v47\n"
    "v_mfma_f32_32x32x16_bf16 v[32:47], v[208:211], v[136:139], 0\n"
    "v_exp_f32 v48, v16\n"
    "v_exp_f32 v49, v17\n"
    "v_exp_f32 v50, v18\n"
    "v_exp_f32 v51, v19\n"
    "v_mfma_f32_32x32x16_bf16 v[32:47], v[212:215], v[140:143], v[32:47]\n"
    "v_exp_f32 v52, v20\n"
    "v_exp_f32 v53, v21\n"
    "v_exp_f32 v54, v22\n"
    "v_exp_f32 v55, v23\n"
    "v_smfmac_f32_32x32x32_bf16 v[64:79], v[220:223], v[56:63], v10\n"
    "v_exp_f32 v56, v24\n"
    "v_exp_f32 v57, v25\n"
    "v_exp_f32 v58, v26\n"
    "v_exp_f32 v59, v27\n"
    "v_smfmac_f32_32x32x32_bf16 v[80:95], v[200:203], v[48:55], v10\n"
    "v_exp_f32 v60, v28\n"
    "v_exp_f32 v61, v29\n"
    "v_exp_f32 v62, v30\n"
    "v_exp_f32 v63, v31\n"
    "v_mfma_f32_32x32x16_bf16 v[16:31], v[192:195], v[144:147], 0\n"
    "v_exp_f32 v48, v32\n"
    "v_exp_f32 v49, v33\n"
    "v_exp_f32 v50, v34\n"
    "v_exp_f32 v51, v35\n"
    "v_mfma_f32_32x32x16_bf16 v[16:31], v[196:199], v[148:151], v[16:31]\n"
    "v_exp_f32 v52, v36\n"
    "v_exp_f32 v53, v37\n"
    "v_exp_f32 v54, v38\n"
    "v_exp_f32 v55, v39\n"
    "v_smfmac_f32_32x32x32_bf16 v[80:95], v[204:207], v[56:63], v10\n"
    "ds_write_b128 v12, v[64:67] offset:20480\n"
    "v_exp_f32 v56, v40\n"
    "v_exp_f32 v57, v41\n"
    "v_exp_f32 v58, v42\n"
    "v_exp_f32 v59, v43\n"
    "v_smfmac_f32_32x32x32_bf16 v[80:95], v[216:219], v[48:55], v10\n"
    "v_exp_f32 v60, v44\n"
    "v_exp_f32 v61, v45\n"
    "v_exp_f32 v62, v46\n"
    "v_exp_f32 v63, v47\n"
    "v_mfma_f32_32x32x16_bf16 v[32:47], v[208:211], v[144:147], 0\n"
    "ds_write_b128 v12, v[68:71] offset:21504\n"
    "v_exp_f32 v48, v16\n"
    "v_exp_f32 v49, v17\n"
    "v_exp_f32 v50, v18\n"
    "v_exp_f32 v51, v19\n"
    "v_mfma_f32_32x32x16_bf16 v[32:47], v[212:215], v[148:151], v[32:47]\n"
    "v_exp_f32 v52, v20\n"
    "v_exp_f32 v53, v21\n"
    "v_exp_f32 v54, v22\n"
    "v_exp_f32 v55, v23\n"
    "v_smfmac_f32_32x32x32_bf16 v[80:95], v[220:223], v[56:63], v10\n"
    "ds_write_b128 v12, v[72:75] offset:22528\n"
    "v_exp_f32 v56, v24\n"
    "v_exp_f32 v57, v25\n"
    "v_exp_f32 v58, v26\n"
    "v_exp_f32 v59, v27\n"
    "v_smfmac_f32_32x32x32_bf16 v[96:111], v[200:203], v[48:55], v10\n"
    "v_exp_f32 v60, v28\n"
    "v_exp_f32 v61, v29\n"
    "v_exp_f32 v62, v30\n"
    "v_exp_f32 v63, v31\n"
    "v_mfma_f32_32x32x16_bf16 v[16:31], v[192:195], v[152:155], 0\n"
    "ds_write_b128 v12, v[76:79] offset:23552\n"
    "v_exp_f32 v48, v32\n"
    "v_exp_f32 v49, v33\n"
    "v_exp_f32 v50, v34\n"
    "v_exp_f32 v51, v35\n"
    "v_mfma_f32_32x32x16_bf16 v[16:31], v[196:199], v[156:159], v[16:31]\n"
    "v_exp_f32 v52, v36\n"
    "v_exp_f32 v53, v37\n"
    "v_exp_f32 v54, v38\n"
    "v_exp_f32 v55, v39\n"
    "v_smfmac_f32_32x32x32_bf16 v[96:111], v[204:207], v[56:63], v10\n"
    "ds_write_b128 v12, v[80:83] offset:24576\n"
    "v_exp_f32 v56, v40\n"
    "v_exp_f32 v57, v41\n"
    "v_exp_f32 v58, v42\n"
    "v_exp_f32 v59, v43\n"
    "v_smfmac_f32_32x32x32_bf16 v[96:111], v[216:219], v[48:55], v10\n"
    "v_exp_f32 v60, v44\n"
    "v_exp_f32 v61, v45\n"
    "v_exp_f32 v62, v46\n"
    "v_exp_f32 v63, v47\n"
    "v_mfma_f32_32x32x16_bf16 v[32:47], v[208:211], v[152:155], 0\n"
    "ds_write_b128 v12, v[84:87] offset:25600\n"
    "v_exp_f32 v48, v16\n"
    "v_exp_f32 v49, v17\n"
    "v_exp_f32 v50, v18\n"
    "v_exp_f32 v51, v19\n"
    "v_mfma_f32_32x32x16_bf16 v[32:47], v[212:215], v[156:159], v[32:47]\n"
    "v_exp_f32 v52, v20\n"
    "v_exp_f32 v53, v21\n"
    "v_exp_f32 v54, v22\n"
    "v_exp_f32 v55, v23\n"
    "v_smfmac_f32_32x32x32_bf16 v[96:111], v[220:223], v[56:63], v10\n"
    "ds_write_b128 v12, v[88:91] offset:26624\n"
    "v_exp_f32 v56, v24\n"
    "v_exp_f32 v57, v25\n"
    "v_exp_f32 v58, v26\n"
    "v_exp_f32 v59, v27\n"
    "v_smfmac_f32_32x32x32_bf16 v[112:127], v[200:203], v[48:55], v10\n"
    "v_exp_f32 v60, v28\n"
    "v_exp_f32 v61, v29\n"
    "v_exp_f32 v62, v30\n"
    "v_exp_f32 v63, v31\n"
    "ds_write_b128 v12, v[92:95] offset:27648\n"
    "v_exp_f32 v48, v32\n"
    "v_exp_f32 v49, v33\n"
    "v_exp_f32 v50, v34\n"
    "v_exp_f32 v51, v35\n"
    "v_exp_f32 v52, v36\n"
    "v_exp_f32 v53, v37\n"
    "v_exp_f32 v54, v38\n"
    "v_exp_f32 v55, v39\n"
    "v_smfmac_f32_32x32x32_bf16 v[112:127], v[204:207], v[56:63], v10\n"
    "ds_write_b128 v12, v[96:99] offset:0\n"
    "v_exp_f32 v56, v40\n"
    "v_exp_f32 v57, v41\n"
    "v_exp_f32 v58, v42\n"
    "v_exp_f32 v59, v43\n"
    "v_smfmac_f32_32x32x32_bf16 v[112:127], v[216:219], v[48:55], v10\n"
    "v_exp_f32 v60, v44\n"
    "v_exp_f32 v61, v45\n"
    "v_exp_f32 v62, v46\n"
    "v_exp_f32 v63, v47\n"
    "s_setprio 3\n"
    "ds_write_b128 v12, v[100:103] offset:1024\n"
    "v_smfmac_f32_32x32x32_bf16 v[112:127], v[220:223], v[56:63], v10\n"
    "ds_write_b128 v12, v[104:107] offset:2048\n"
    "ds_write_b128 v12, v[108:111] offset:3072\n"
    "s_waitcnt lgkmcnt(0)\n"
    "s_barrier\n"
    "ds_read_b128 v[16:19], v241 offset:0\n"
    "ds_read_b128 v[20:23], v241 offset:1024\n"
    "ds_read_b128 v[24:27], v241 offset:2048\n"
    "ds_read_b128 v[28:31], v241 offset:3072\n"
    "ds_read_b128 v[32:35], v242 offset:0\n"
    "ds_read_b128 v[36:39], v242 offset:1024\n"
    "ds_read_b128 v[40:43], v242 offset:2048\n"
    "ds_read_b128 v[44:47], v242 offset:3072\n"
    "ds_read_b128 v[48:51], v243 offset:0\n"
    "ds_read_b128 v[52:55], v243 offset:1024\n"
    "ds_read_b128 v[56:59], v243 offset:2048\n"
    "ds_read_b128 v[60:63], v243 offset:3072\n"
    "s_waitcnt lgkmcnt(8)\n"
    "v_add_f32 v112, v112, v16\n"
    "v_add_f32 v113, v113, v17\n"
    "v_add_f32 v114, v114, v18\n"
    "v_add_f32 v115, v115, v19\n"
    "v_add_f32 v116, v116, v20\n"
    "v_add_f32 v117, v117, v21\n"
    "v_add_f32 v118, v118, v22\n"
    "v_add_f32 v119, v119, v23\n"
    "v_add_f32 v120, v120, v24\n"
    "v_add_f32 v121, v121, v25\n"
    "v_add_f32 v122, v122, v26\n"
    "v_add_f32 v123, v123, v27\n"
    "v_add_f32 v124, v124, v28\n"
    "v_add_f32 v125, v125, v29\n"
    "v_add_f32 v126, v126, v30\n"
    "v_add_f32 v127, v127, v31\n"
    "s_waitcnt lgkmcnt(4)\n"
    "v_add_f32 v112, v112, v32\n"
    "v_add_f32 v113, v113, v33\n"
    "v_add_f32 v114, v114, v34\n"
    "v_add_f32 v115, v115, v35\n"
    "v_add_f32 v116, v116, v36\n"
    "v_add_f32 v117, v117, v37\n"
    "v_add_f32 v118, v118, v38\n"
    "v_add_f32 v119, v119, v39\n"
    "v_add_f32 v120, v120, v40\n"
    "v_add_f32 v121, v121, v41\n"
    "v_add_f32 v122, v122, v42\n"
    "v_add_f32 v123, v123, v43\n"
    "v_add_f32 v124, v124, v44\n"
    "v_add_f32 v125, v125, v45\n"
    "v_add_f32 v126, v126, v46\n"
    "v_add_f32 v127, v127, v47\n"
    "s_waitcnt lgkmcnt(0)\n"
    "v_add_f32 v112, v112, v48\n"
    "v_add_f32 v113, v113, v49\n"
    "v_add_f32 v114, v114, v50\n"
    "v_add_f32 v115, v115, v51\n"
    "v_add_f32 v116, v116, v52\n"
    "v_add_f32 v117, v117, v53\n"
    "v_add_f32 v118, v118, v54\n"
    "v_add_f32 v119, v119, v55\n"
    "v_add_f32 v120, v120, v56\n"
    "v_add_f32 v121, v121, v57\n"
    "v_add_f32 v122, v122, v58\n"
    "v_add_f32 v123, v123, v59\n"
    "v_add_f32 v124, v124, v60\n"
    "v_add_f32 v125, v125, v61\n"
    "v_add_f32 v126, v126, v62\n"
    "v_add_f32 v127, v127, v63\n"
    "v_add_f32 v127, v127, v119\n"
    "v_cmp_nle_f32 s[62:63], 1.0, v119\n"
    "v_cmp_ngt_f32 vcc, 0x5f800000, v127\n"
    "s_or_b64 vcc, vcc, s[62:63]\n"
    "s_cbranch_vccnz .L_FALL\n"
    ".L_NORM:\n"
    "v_rcp_f32 v16, v119\n"
    "s_nop 0\n"
    "v_mul_f32 v112, v112, v16\n"
    "v_mul_f32 v113, v113, v16\n"
    "v_mul_f32 v114, v114, v16\n"
    "v_mul_f32 v115, v115, v16\n"
    "v_mul_f32 v116, v116, v16\n"
    "v_mul_f32 v117, v117, v16\n"
    "v_mul_f32 v118, v118, v16\n"
    "v_cvt_pk_bf16_f32 v20, v112, v113\n"
    "v_cvt_pk_bf16_f32 v21, v114, v115\n"
    "v_cvt_pk_bf16_f32 v22, v116, v117\n"
    "v_cvt_pk_bf16_f32 v23, v118, 0\n"
    "v_min_u32 v20, -2, v20\n"
    "v_min_u32 v21, -2, v21\n"
    "v_min_u32 v22, -2, v22\n"
    "buffer_store_dwordx4 v[20:23], v244, s[52:55], s88 offen offset:0 sc1\n"
    "v_mul_f32 v120, v120, v16\n"
    "v_mul_f32 v121, v121, v16\n"
    "v_mul_f32 v122, v122, v16\n"
    "v_mul_f32 v123, v123, v16\n"
    "v_mul_f32 v124, v124, v16\n"
    "v_mul_f32 v125, v125, v16\n"
    "v_mul_f32 v126, v126, v16\n"
    "v_cvt_pk_bf16_f32 v24, v120, v121\n"
    "v_cvt_pk_bf16_f32 v25, v122, v123\n"
    "v_cvt_pk_bf16_f32 v26, v124, v125\n"
    "v_cvt_pk_bf16_f32 v27, v126, 0\n"
    "v_min_u32 v24, -2, v24\n"
    "v_min_u32 v25, -2, v25\n"
    "v_min_u32 v26, -2, v26\n"
    "buffer_store_dwordx4 v[24:27], v244, s[52:55], s88 offen offset:1024 sc1\n"
    "v_add_u32 v56, s80, v244\n"
    "v_add_u32 v57, s81, v245\n"
    "v_add_u32 v58, s82, v246\n"
    "v_add_u32 v59, s83, v247\n"
    "ds_write_b128 v56, v[20:23] offset:4096\n"
    "ds_write_b128 v56, v[24:27] offset:5120\n"
    "s_lshr_b32 s59, s32, 1\n"
    "s_lshl_b32 s58, s36, 5\n"
    "s_add_u32 s58, s58, s35\n"
    "s_lshl1_add_u32 s58, s58, s59\n"
    "s_lshl_b32 s58, s58, 6\n"
    "s_add_u32 s58, s58, s64\n"
    "s_lshl_b32 s58, s58, 6\n"
    "s_add_u32 s89, s58, 3145728\n"
    "v_lshlrev_b32 v248, 8, v5\n"
    "v_lshl_add_u32 v248, v6, 4, v248\n"
    "s_mov_b32 s78, 0x05040100\n"
    "s_mov_b32 s79, 0x07060302\n"
    "s_setprio 1\n"
    "s_sleep 20\n"
    "s_mov_b32 s60, 1048576\n"
    ".L_P3:\n"
    "buffer_load_dwordx4 v[32:35], v245, s[52:55], s88 offen offset:0 sc1\n"
    "buffer_load_dwordx4 v[36:39], v245, s[52:55], s88 offen offset:1024 sc1\n"
    "buffer_load_dwordx4 v[40:43], v246, s[52:55], s88 offen offset:0 sc1\n"
    "buffer_load_dwordx4 v[44:47], v246, s[52:55], s88 offen offset:1024 sc1\n"
    "buffer_load_dwordx4 v[48:51], v247, s[52:55], s88 offen offset:0 sc1\n"
    "buffer_load_dwordx4 v[52:55], v247, s[52:55], s88 offen offset:1024 sc1\n"
    "s_waitcnt vmcnt(0)\n"
    "v_max3_u32 v7, v32, v33, v34\n"
    "v_max3_u32 v7, v7, v35, v36\n"
    "v_max3_u32 v7, v7, v37, v38\n"
    "v_max3_u32 v7, v7, v39, v40\n"
    "v_max3_u32 v7, v7, v41, v42\n"
    "v_max3_u32 v7, v7, v43, v44\n"
    "v_max3_u32 v7, v7, v45, v46\n"
    "v_max3_u32 v7, v7, v47, v48\n"
    "v_max3_u32 v7, v7, v49, v50\n"
    "v_max3_u32 v7, v7, v51, v52\n"
    "v_max3_u32 v7, v7, v53, v54\n"
    "v_max_u32 v7, v7, v55\n"
    "v_cmp_eq_u32 vcc, -1, v7\n"
    "s_cbranch_vccz .L_P3OK\n"
    "s_sub_u32 s60, s60, 1\n"
    "s_cbranch_scc0 .L_P3\n"
    ".L_P3OK:\n"
    "ds_write_b128 v57, v[32:35] offset:4096\n"
    "ds_write_b128 v57, v[36:39] offset:5120\n"
    "ds_write_b128 v58, v[40:43] offset:4096\n"
    "ds_write_b128 v58, v[44:47] offset:5120\n"
    "ds_write_b128 v59, v[48:51] offset:4096\n"
    "ds_write_b128 v59, v[52:55] offset:5120\n"
    "s_waitcnt lgkmcnt(0)\n"
    "s_barrier\n"
    "s_cmp_eq_u32 s42, 0\n"
    "s_cbranch_scc1 .L_END\n"
    "ds_read_b128 v[64:67], v12 offset:12288\n"
    "ds_read_b128 v[68:71], v12 offset:13312\n"
    "ds_read_b64 v[72:73], v12 offset:14336\n"
    "s_sleep 10\n"
    "s_mov_b32 s60, 1048576\n"
    ".L_PY:\n"
    "buffer_load_dwordx4 v[48:51], v248, s[52:55], s89 offen sc1\n"
    "buffer_load_dwordx4 v[52:55], v248, s[52:55], s89 offen offset:128 sc1\n"
    "s_waitcnt vmcnt(0) lgkmcnt(0)\n"
    "v_xnor_b32 v48, v48, v64\n"
    "v_xnor_b32 v49, v49, v65\n"
    "v_xnor_b32 v50, v50, v66\n"
    "v_xnor_b32 v51, v51, v67\n"
    "v_xnor_b32 v52, v52, v68\n"
    "v_xnor_b32 v53, v53, v69\n"
    "v_xnor_b32 v54, v54, v70\n"
    "v_xnor_b32 v55, v55, v71\n"
    "s_waitcnt vmcnt(0)\n"
    "v_max3_u32 v7, v48, v49, v50\n"
    "v_max3_u32 v7, v7, v51, v52\n"
    "v_max3_u32 v7, v7, v53, v54\n"
    "v_max_u32 v7, v7, v55\n"
    "v_cmp_eq_u32 vcc, -1, v7\n"
    "s_cbranch_vccz .L_PYOK\n"
    "s_sub_u32 s60, s60, 1\n"
    "s_cbranch_scc0 .L_PY\n"
    ".L_PYOK:\n"
    "v_perm_b32 v56, v52, v48, s78\n"
    "global_store_dword v72, v56, s[12:13] offset:-3584 sc1\n"
    "v_perm_b32 v57, v52, v48, s79\n"
    "global_store_dword v72, v57, s[12:13] offset:-1792 sc1\n"
    "v_perm_b32 v58, v53, v49, s78\n"
    "global_store_dword v72, v58, s[12:13] offset:0 sc1\n"
    "v_perm_b32 v59, v53, v49, s79\n"
    "global_store_dword v72, v59, s[12:13] offset:1792 sc1\n"
    "v_perm_b32 v60, v54, v50, s78\n"
    "global_store_dword v73, v60, s[12:13] offset:-3584 sc1\n"
    "v_perm_b32 v61, v54, v50, s79\n"
    "global_store_dword v73, v61, s[12:13] offset:-1792 sc1\n"
    "v_perm_b32 v62, v55, v51, s78\n"
    "global_store_dword v73, v62, s[12:13] offset:0 sc1\n"
    "v_perm_b32 v63, v55, v51, s79\n"
    "global_store_dword v73, v63, s[12:13] offset:1792 sc1\n"
    ".L_END:\n"
    "s_endpgm\n"
    ".L_SL10:\n"
    "s_mov_b32 s60, 1048576\n"
    ".L_SW10:\n"
    "ds_read_b32 v252, v250 offset:160\n"
    "s_waitcnt lgkmcnt(0)\n"
    "v_cmp_eq_u32 vcc, 0, v252\n"
    "s_cbranch_vccz .L_SW10OK\n"
    "s_sub_u32 s60, s60, 1\n"
    "s_cbranch_scc0 .L_SW10\n"
    ".L_SW10OK:\n"
    "s_branch .L_BK10\n"
    ".L_SL11:\n"
    "s_mov_b32 s60, 1048576\n"
    ".L_SW11:\n"
    "ds_read_b32 v252, v250 offset:176\n"
    "s_waitcnt lgkmcnt(0)\n"
    "v_cmp_eq_u32 vcc, 0, v252\n"
    "s_cbranch_vccz .L_SW11OK\n"
    "s_sub_u32 s60, s60, 1\n"
    "s_cbranch_scc0 .L_SW11\n"
    ".L_SW11OK:\n"
    "s_branch .L_BK11\n"
    ".L_SL12:\n"
    "s_mov_b32 s60, 1048576\n"
    ".L_SW12:\n"
    "ds_read_b32 v252, v250 offset:192\n"
    "s_waitcnt lgkmcnt(0)\n"
    "v_cmp_eq_u32 vcc, 0, v252\n"
    "s_cbranch_vccz .L_SW12OK\n"
    "s_sub_u32 s60, s60, 1\n"
    "s_cbranch_scc0 .L_SW12\n"
    ".L_SW12OK:\n"
    "s_branch .L_BK12\n"
    ".L_SL13:\n"
    "s_mov_b32 s60, 1048576\n"
    ".L_SW13:\n"
    "ds_read_b32 v252, v250 offset:208\n"
    "s_waitcnt lgkmcnt(0)\n"
    "v_cmp_eq_u32 vcc, 0, v252\n"
    "s_cbranch_vccz .L_SW13OK\n"
    "s_sub_u32 s60, s60, 1\n"
    "s_cbranch_scc0 .L_SW13\n"
    ".L_SW13OK:\n"
    "s_branch .L_BK13\n"
    ".L_SL14:\n"
    "s_mov_b32 s60, 1048576\n"
    ".L_SW14:\n"
    "ds_read_b32 v252, v250 offset:224\n"
    "s_waitcnt lgkmcnt(0)\n"
    "v_cmp_eq_u32 vcc, 0, v252\n"
    "s_cbranch_vccz .L_SW14OK\n"
    "s_sub_u32 s60, s60, 1\n"
    "s_cbranch_scc0 .L_SW14\n"
    ".L_SW14OK:\n"
    "s_branch .L_BK14\n"
    ".L_HPRE:\n"
    "v_and_b32 v9, 255, v0\n"
    "s_mul_i32 s58, s63, 17408\n"
    "s_lshl_b32 s59, s64, 10\n"
    "v_add_u32 v149, s58, v2\n"
    "v_add_u32 v150, s59, v149\n"
    "s_mul_i32 s58, s63, 1552\n"
    "s_add_u32 s58, s58, 158224\n"
    "v_lshl_add_u32 v148, v6, 7, s58\n"
    "v_lshl_add_u32 v148, v5, 1, v148\n"
    "s_cmp_eq_u32 s64, 0\n"
    "s_cselect_b32 s70, 2, 7\n"
    "s_cselect_b32 s71, 4, 3\n"
    "s_cselect_b32 s72, 256, 2\n"
    "s_cselect_b32 s73, 2, 4\n"
    "s_cselect_b32 s74, 0, 60\n"
    "s_lshl_b32 s59, s64, 9\n"
    "s_add_u32 s58, s58, s59\n"
    "v_and_b32 v7, s64, v4\n"
    "v_sub_u32 v7, s71, v7\n"
    "v_lshlrev_b32 v7, v7, v5\n"
    "v_lshlrev_b32 v144, s70, v6\n"
    "v_add3_u32 v144, v144, v7, s58\n"
    "v_add_u32 v145, s72, v144\n"
    "v_mov_b32 v7, s73\n"
    "v_mad_u32_u24 v7, v4, s74, v7\n"
    "v_add_u32 v146, v7, v144\n"
    "v_add_u32 v147, v7, v145\n"
    "v_lshlrev_b32 v8, 3, v9\n"
    "v_sub_u32 v9, 131, v8\n"
    "v_max_i32 v9, 0, v9\n"
    "v_add3_u32 v60, v8, v9, -3\n"
    "v_cvt_f32_u32 v60, v60\n"
    "v_lshlrev_b32 v9, 1, v9\n"
    "v_sub_u32 v9, 124176, v9\n"
    "v_add_u32 v143, 114720, v8\n"
    "v_add_f32 v61, 1.0, v60\n"
    "v_add_f32 v62, 1.0, v61\n"
    "v_add_f32 v63, 1.0, v62\n"
    "v_add_f32 v64, 1.0, v63\n"
    "v_add_f32 v65, 1.0, v64\n"
    "v_add_f32 v66, 1.0, v65\n"
    "v_add_f32 v67, 1.0, v66\n"
    "v_add_f32 v68, 1.0, v67\n"
    "v_add_f32 v69, 1.0, v68\n"
    "v_add_f32 v70, 1.0, v69\n"
    "v_cvt_pk_bf16_f32 v60, v60, v61\n"
    "v_cvt_pk_bf16_f32 v62, v62, v63\n"
    "v_cvt_pk_bf16_f32 v64, v64, v65\n"
    "v_cvt_pk_bf16_f32 v66, v66, v67\n"
    "v_cvt_pk_bf16_f32 v68, v68, v69\n"
    "v_cvt_pk_bf16_f32 v70, v70, v70\n"
    "v_mad_u32_u16 v132, v60, 2, v9\n"
    "v_mad_u32_u16 v133, v60, 2, v9 op_sel:[1,0,0,0]\n"
    "v_mad_u32_u16 v134, v62, 2, v9\n"
    "v_mad_u32_u16 v135, v62, 2, v9 op_sel:[1,0,0,0]\n"
    "v_mad_u32_u16 v136, v64, 2, v9\n"
    "v_mad_u32_u16 v137, v64, 2, v9 op_sel:[1,0,0,0]\n"
    "v_mad_u32_u16 v138, v66, 2, v9\n"
    "v_mad_u32_u16 v139, v66, 2, v9 op_sel:[1,0,0,0]\n"
    "v_mad_u32_u16 v140, v68, 2, v9\n"
    "v_mad_u32_u16 v141, v68, 2, v9 op_sel:[1,0,0,0]\n"
    "v_mad_u32_u16 v142, v70, 2, v9\n"
    "v_lshlrev_b32 v248, 8, v5\n"
    "v_lshl_add_u32 v248, v6, 4, v248\n"
    "v_add_u32 v13, 57344, v2\n"
    "s_mul_i32 s58, s43, 28672\n"
    "v_add_u32 v22, s58, v2\n"
    "v_lshlrev_b32 v17, 7, v3\n"
    "s_lshr_b32 s58, s37, 5\n"
    "s_lshl_b32 s58, s58, 12\n"
    "s_add_u32 s58, s58, 45056\n"
    "v_add_u32 v16, s58, v17\n"
    "s_lshl_b32 s58, s42, 5\n"
    "s_add_u32 s58, s58, s33\n"
    "s_lshl_b32 s58, s58, 7\n"
    "s_add_u32 s58, s58, 90112\n"
    "v_and_b32 v23, 3, v0\n"
    "v_lshl_add_u32 v23, v23, 10, s58\n"
    "s_lshl2_add_u32 s86, s39, s43\n"
    "s_mul_i32 s58, s38, 57344\n"
    "s_lshl_b32 s59, s86, 6\n"
    "s_add_u32 s58, s58, s59\n"
    "v_mul_u32_u24 v249, 1792, v3\n"
    "v_lshl_add_u32 v249, v4, 4, v249\n"
    "v_add_u32 v249, s58, v249\n"
    "v_lshrrev_b32 v15, 3, v1\n"
    "v_lshlrev_b32 v15, 13, v15\n"
    "v_and_b32 v8, 7, v0\n"
    "v_lshl_add_u32 v15, v8, 4, v15\n"
    "s_lshr_b32 s58, s35, 4\n"
    "s_lshl_b32 s58, s58, 2\n"
    "s_and_b32 s59, s35, 1\n"
    "s_add_u32 s58, s58, s59\n"
    "s_lshl1_add_u32 s58, s58, s64\n"
    "s_lshl_b32 s58, s58, 6\n"
    "s_and_b32 s59, s35, 14\n"
    "s_add_u32 s58, s58, s59\n"
    "s_mul_i32 s58, s58, 1792\n"
    "s_lshl1_add_u32 s59, s36, s63\n"
    "s_lshl_b32 s59, s59, 6\n"
    "v_mul_u32_u24 v7, 28672, v6\n"
    "v_add_u32 v7, s58, v7\n"
    "v_lshl_add_u32 v8, v5, 2, s59\n"
    "v_add_u32 v18, v7, v8\n"
    "v_add_u32 v19, 458752, v18\n"
    "v_lshl_add_u32 v20, v7, 1, v8\n"
    "v_add_u32 v20, 4480, v20\n"
    "v_add_u32 v21, 917504, v20\n"
    "s_lshl_b32 s62, s37, 2\n"
    "s_waitcnt lgkmcnt(0)\n"
    "s_load_dword s3, s[24:25], s62\n"
    "s_load_dword s61, s[26:27], s62\n"
    "s_load_dword s40, s[52:53], 0x0\n"
    "s_setprio 2\n"
    "s_branch .L_HPB\n"
    ".L_HT:\n"
    "s_waitcnt vmcnt(12)\n"
    "s_barrier\n"
    "s_lshl_b32 s58, s64, 4\n"
    "s_add_u32 s58, s58, 32\n"
    "v_lshlrev_b32 v7, 3, v6\n"
    "v_sub_u32 v7, v5, v7\n"
    "v_add_u32 v7, s58, v7\n"
    "v_add_u32 v8, -4, v7\n"
    "s_mul_i32 s58, s63, 16640\n"
    "s_add_u32 s58, s58, 114688\n"
    "v_and_b32 v10, 7, v7\n"
    "v_and_b32 v11, 7, v8\n"
    "v_mul_u32_u24 v10, 2080, v10\n"
    "v_mul_u32_u24 v11, 2080, v11\n"
    "v_and_b32 v7, -8, v7\n"
    "v_and_b32 v8, -8, v8\n"
    "v_add3_u32 v10, v10, v7, s58\n"
    "v_add3_u32 v11, v11, v8, s58\n"
    "s_mul_i32 s58, s63, 5120\n"
    "s_add_u32 s58, s58, 148096\n"
    "v_lshl_add_u32 v7, v5, 4, s58\n"
    "v_mul_u32_u24 v12, 1280, v6\n"
    "v_add_u32 v12, v12, v7\n"
    "s_mul_i32 s58, s64, 2560\n"
    "s_add_u32 s58, s58, 128\n"
    "v_mul_u32_u24 v14, 1280, v4\n"
    "v_add3_u32 v14, v14, v7, s58\n"
    "v_and_b32 v8, 1, v6\n"
    "v_lshl_add_u32 v14, v8, 3, v14\n"
    "s_waitcnt vmcnt(8)\n"
    "s_barrier\n"
    "s_waitcnt vmcnt(4)\n"
    "s_barrier\n"
    ".L_HT2:\n"
    "s_mul_i32 s41, s43, 28672\n"
    "s_lshl_b32 s57, s43, 16\n"
    "s_add_u32 s57, s57, 147456\n"
    "s_waitcnt lgkmcnt(0)\n"
    "s_mul_i32 s40, s40, 0x19660d\n"
    "s_add_u32 s40, s40, 0x3c6ef35f\n"
    "s_and_b32 s3, s3, 0xffff0000\n"
    "v_mov_b32 v53, s3\n"
    "s_lshl_b32 s58, s63, 4\n"
    "s_lshr_b32 s61, s61, s58\n"
    "s_lshl_b32 s61, s61, 16\n"
    "s_cmp_eq_u32 s64, 0\n"
    "s_cselect_b64 vcc, 1, 0\n"
    "v_mov_b32 v151, s61\n"
    "v_cndmask_b32 v151, 0, v151, vcc\n"
    "s_cmp_eq_u32 s42, 0\n"
    "s_barrier\n"
    "s_cbranch_scc1 .L_HK\n"
    "s_waitcnt vmcnt(0)\n"
    "s_branch .L_KG\n"
    ".L_H2:\n"
    "v_mov_b64 v[24:25], 0\n"
    "v_mov_b64 v[26:27], 0\n"
    "v_mov_b64 v[28:29], 0\n"
    "v_mov_b64 v[30:31], 0\n"
    "s_nop 1\n"
    "ds_write_b128 v34, v[228:231] offset:2048\n"
    "ds_write_b32 v250, v250 offset:128\n"
    "v_and_b32 v7, 7, v0\n"
    "v_lshlrev_b32 v7, 2, v7\n"
    "v_add_u32 v7, 161312, v7\n"
    "s_mov_b32 s60, 1048576\n"
    ".L_SP:\n"
    "ds_read_b32 v252, v7 offset:112\n"
    "s_waitcnt lgkmcnt(0)\n"
    "v_cmp_eq_u32 vcc, 0, v252\n"
    "s_cbranch_vccz .L_SPOK\n"
    "s_sub_u32 s60, s60, 1\n"
    "s_cbranch_scc0 .L_SP\n"
    ".L_SPOK:\n"
    "ds_read_b128 v[68:71], v150 offset:0\n"
    "ds_read_b128 v[84:87], v149 offset:2048\n"
    "ds_read_b128 v[72:75], v150 offset:4352\n"
    "ds_read_b128 v[88:91], v149 offset:6400\n"
    "ds_read_b128 v[76:79], v150 offset:8704\n"
    "ds_read_b128 v[92:95], v149 offset:10752\n"
    "ds_read_b128 v[80:83], v150 offset:13056\n"
    "ds_read_b128 v[96:99], v149 offset:15104\n"
    "v_mov_b64 v[32:33], 0\n"
    "v_mov_b64 v[34:35], 0\n"
    "v_mov_b64 v[36:37], 0\n"
    "v_mov_b64 v[38:39], 0\n"
    "s_waitcnt lgkmcnt(4)\n"
    "v_add_f32 v68, v68, v72\n"
    "v_add_f32 v69, v69, v73\n"
    "v_add_f32 v70, v70, v74\n"
    "v_add_f32 v71, v71, v75\n"
    "v_add_f32 v84, v84, v88\n"
    "v_add_f32 v85, v85, v89\n"
    "v_add_f32 v86, v86, v90\n"
    "v_add_f32 v87, v87, v91\n"
    "s_waitcnt lgkmcnt(2)\n"
    "v_add_f32 v68, v68, v76\n"
    "v_add_f32 v69, v69, v77\n"
    "v_add_f32 v70, v70, v78\n"
    "v_add_f32 v71, v71, v79\n"
    "v_add_f32 v84, v84, v92\n"
    "v_add_f32 v85, v85, v93\n"
    "v_add_f32 v86, v86, v94\n"
    "v_add_f32 v87, v87, v95\n"
    "s_waitcnt lgkmcnt(0)\n"
    "v_add_f32 v68, v68, v80\n"
    "v_add_f32 v69, v69, v81\n"
    "v_add_f32 v70, v70, v82\n"
    "v_add_f32 v71, v71, v83\n"
    "v_add_f32 v84, v84, v96\n"
    "v_add_f32 v85, v85, v97\n"
    "v_add_f32 v86, v86, v98\n"
    "v_add_f32 v87, v87, v99\n"
    "v_add_f32 v68, v68, v151\n"
    "v_cvt_pk_bf16_f32 v84, v84, v85\n"
    "v_cvt_pk_bf16_f32 v86, v86, v87\n"
    "v_cvt_pk_bf16_f32 v68, v68, v69\n"
    "v_cvt_pk_bf16_f32 v70, v70, v71\n"
    "ds_write_b16 v148, v84 offset:1024\n"
    "ds_write_b16_d16_hi v148, v84 offset:1056\n"
    "ds_write_b16 v148, v86 offset:1088\n"
    "ds_write_b16_d16_hi v148, v86 offset:1120\n"
    "ds_write_b16 v144, v68\n"
    "ds_write_b16_d16_hi v145, v68\n"
    "ds_write_b16 v146, v70\n"
    "ds_write_b16_d16_hi v147, v70\n"
    "ds_write_b32 v250, v250 offset:32\n"
    "s_mov_b32 s60, 1048576\n"
    ".L_SY2:\n"
    "ds_read_b32 v252, v251 offset:32\n"
    "s_waitcnt lgkmcnt(0)\n"
    "v_cmp_eq_u32 vcc, 0, v252\n"
    "s_cbranch_vccz .L_SY2OK\n"
    "s_sub_u32 s60, s60, 1\n"
    "s_cbranch_scc0 .L_SY2\n"
    ".L_SY2OK:\n"
    "ds_read_u16 v60, v132 offset:0\n"
    "ds_read_u16 v61, v133 offset:0\n"
    "ds_read_u16 v62, v134 offset:0\n"
    "ds_read_u16 v63, v135 offset:0\n"
    "ds_read_u16 v64, v136 offset:0\n"
    "ds_read_u16 v65, v137 offset:0\n"
    "ds_read_u16 v66, v138 offset:0\n"
    "ds_read_u16 v67, v139 offset:0\n"
    "ds_read_u16 v68, v140 offset:0\n"
    "ds_read_u16 v69, v141 offset:0\n"
    "ds_read_u16 v70, v142 offset:0\n"
    "ds_read_u16 v72, v132 offset:1552\n"
    "ds_read_u16 v73, v133 offset:1552\n"
    "ds_read_u16 v74, v134 offset:1552\n"
    "ds_read_u16 v75, v135 offset:1552\n"
    "ds_read_u16 v76, v136 offset:1552\n"
    "ds_read_u16 v77, v137 offset:1552\n"
    "ds_read_u16 v78, v138 offset:1552\n"
    "ds_read_u16 v79, v139 offset:1552\n"
    "ds_read_u16 v80, v140 offset:1552\n"
    "ds_read_u16 v81, v141 offset:1552\n"
    "ds_read_u16 v82, v142 offset:1552\n"
    "s_waitcnt lgkmcnt(11)\n"
    "v_lshl_or_b32 v84, v62, 16, v63\n"
    "v_lshl_or_b32 v85, v60, 16, v61\n"
    "ds_write_b64 v143, v[84:85] offset:0\n"
    "v_lshl_or_b32 v86, v63, 16, v64\n"
    "v_lshl_or_b32 v87, v61, 16, v62\n"
    "ds_write_b64 v143, v[86:87] offset:2080\n"
    "v_lshl_or_b32 v88, v64, 16, v65\n"
    "v_lshl_or_b32 v89, v62, 16, v63\n"
    "ds_write_b64 v143, v[88:89] offset:4160\n"
    "v_lshl_or_b32 v90, v65, 16, v66\n"
    "v_lshl_or_b32 v91, v63, 16, v64\n"
    "ds_write_b64 v143, v[90:91] offset:6240\n"
    "v_lshl_or_b32 v84, v66, 16, v67\n"
    "v_lshl_or_b32 v85, v64, 16, v65\n"
    "ds_write_b64 v143, v[84:85] offset:8320\n"
    "v_lshl_or_b32 v86, v67, 16, v68\n"
    "v_lshl_or_b32 v87, v65, 16, v66\n"
    "ds_write_b64 v143, v[86:87] offset:10400\n"
    "v_lshl_or_b32 v88, v68, 16, v69\n"
    "v_lshl_or_b32 v89, v66, 16, v67\n"
    "ds_write_b64 v143, v[88:89] offset:12480\n"
    "v_lshl_or_b32 v90, v69, 16, v70\n"
    "v_lshl_or_b32 v91, v67, 16, v68\n"
    "ds_write_b64 v143, v[90:91] offset:14560\n"
    "s_waitcnt lgkmcnt(8)\n"
    "v_lshl_or_b32 v84, v74, 16, v75\n"
    "v_lshl_or_b32 v85, v72, 16, v73\n"
    "ds_write_b64 v143, v[84:85] offset:16640\n"
    "v_lshl_or_b32 v86, v75, 16, v76\n"
    "v_lshl_or_b32 v87, v73, 16, v74\n"
    "ds_write_b64 v143, v[86:87] offset:18720\n"
    "v_lshl_or_b32 v88, v76, 16, v77\n"
    "v_lshl_or_b32 v89, v74, 16, v75\n"
    "ds_write_b64 v143, v[88:89] offset:20800\n"
    "v_lshl_or_b32 v90, v77, 16, v78\n"
    "v_lshl_or_b32 v91, v75, 16, v76\n"
    "ds_write_b64 v143, v[90:91] offset:22880\n"
    "v_lshl_or_b32 v84, v78, 16, v79\n"
    "v_lshl_or_b32 v85, v76, 16, v77\n"
    "ds_write_b64 v143, v[84:85] offset:24960\n"
    "v_lshl_or_b32 v86, v79, 16, v80\n"
    "v_lshl_or_b32 v87, v77, 16, v78\n"
    "ds_write_b64 v143, v[86:87] offset:27040\n"
    "v_lshl_or_b32 v88, v80, 16, v81\n"
    "v_lshl_or_b32 v89, v78, 16, v79\n"
    "ds_write_b64 v143, v[88:89] offset:29120\n"
    "v_lshl_or_b32 v90, v81, 16, v82\n"
    "v_lshl_or_b32 v91, v79, 16, v80\n"
    "ds_write_b64 v143, v[90:91] offset:31200\n"
    ".L_H3K:\n"
    "v_mov_b64 v[56:57], -1\n"
    "v_mov_b64 v[58:59], -1\n"
    "s_mov_b32 s65, 0\n"
    "s_mov_b32 s60, 1048576\n"
    "ds_read_b32 v252, v251 offset:144\n"
    "s_branch .L_SQC\n"
    ".L_SQ:\n"
    "s_sleep 1\n"
    "ds_read_b32 v252, v251 offset:144\n"
    ".L_SQC:\n"
    "s_waitcnt lgkmcnt(0)\n"
    "v_cmp_eq_u32 vcc, 0, v252\n"
    "s_cbranch_vccz .L_SQOK\n"
    "s_sub_u32 s60, s60, 1\n"
    "s_cbranch_scc0 .L_SQ\n"
    ".L_SQOK:\n"
    "s_add_u32 s56, s57, 12288\n"
    ".L_R1:\n"
    "s_mov_b32 m0, s41\n"
    "s_add_u32 s41, s41, 4096\n"
    "buffer_load_dwordx4 v2, s[52:55], s57 offen offset:0 sc1 lds\n"
    "buffer_load_dwordx4 v2, s[52:55], s57 offen offset:1024 sc1 lds\n"
    "buffer_load_dwordx4 v2, s[52:55], s57 offen offset:2048 sc1 lds\n"
    "buffer_load_dwordx4 v2, s[52:55], s57 offen offset:3072 sc1 lds\n"
    "s_add_u32 s57, s57, 4096\n"
    "s_cmp_lg_u32 s57, s56\n"
    "s_cbranch_scc1 .L_R1\n"
    "s_lshl1_add_u32 s58, s37, s63\n"
    "s_lshr_b32 s59, s58, 6\n"
    "s_and_b32 s58, s58, 63\n"
    "s_lshl2_add_u32 s59, s59, s64\n"
    "s_lshl_b32 s59, s59, 9\n"
    "s_add_u32 s58, s58, s59\n"
    "s_lshl_b32 s58, s58, 7\n"
    "s_add_u32 s84, s58, 3145728\n"
    "s_add_u32 s85, s84, 131072\n"
    "s_lshl_b32 s58, s36, 5\n"
    "s_add_u32 s58, s58, s35\n"
    "s_lshl1_add_u32 s58, s58, s63\n"
    "s_lshl_b32 s58, s58, 6\n"
    "s_add_u32 s58, s58, s64\n"
    "s_lshl_b32 s58, s58, 6\n"
    "s_add_u32 s83, s58, 3145728\n"
    "s_mov_b32 s78, 0x05040100\n"
    "s_mov_b32 s79, 0x07060302\n"
    "s_lshl2_add_u32 s80, s34, s43\n"
    "s_mul_i32 s80, s80, 3584\n"
    "s_add_u32 s80, s80, 524288\n"
    "s_mov_b32 s81, 77824\n"
    "s_lshl_b32 s58, s86, 11\n"
    "s_lshl_b32 s59, s38, 15\n"
    "s_add_u32 s58, s58, s59\n"
    "s_add_u32 s82, s58, 5242880\n"
    "s_min_u32 s87, s86, 13\n"
    "s_lshl_b32 s87, s87, 15\n"
    "s_add_u32 s87, s87, 524288\n"
    "s_waitcnt vmcnt(4)\n"
    "ds_write_b32 v250, v250 offset:160\n"
    "s_add_u32 s56, s57, 4096\n"
    ".L_R2:\n"
    "s_mov_b32 m0, s41\n"
    "s_add_u32 s41, s41, 4096\n"
    "buffer_load_dwordx4 v2, s[52:55], s57 offen offset:0 sc1 lds\n"
    "buffer_load_dwordx4 v2, s[52:55], s57 offen offset:1024 sc1 lds\n"
    "buffer_load_dwordx4 v2, s[52:55], s57 offen offset:2048 sc1 lds\n"
    "buffer_load_dwordx4 v2, s[52:55], s57 offen offset:3072 sc1 lds\n"
    "s_add_u32 s57, s57, 4096\n"
    "s_cmp_lg_u32 s57, s56\n"
    "s_cbranch_scc1 .L_R2\n"
    "s_cmp_eq_u32 s42, 0\n"
    "s_cbranch_scc1 .L_H4\n"
    "global_load_dword v152, v18, s[14:15] offset:0\n"
    "global_load_dword v153, v18, s[14:15] offset:896\n"
    "global_load_dword v154, v18, s[14:15] offset:1792\n"
    "global_load_dword v155, v18, s[14:15] offset:2688\n"
    "global_load_dword v156, v19, s[14:15] offset:0\n"
    "global_load_dword v157, v19, s[14:15] offset:896\n"
    "global_load_dword v158, v19, s[14:15] offset:1792\n"
    "global_load_dword v159, v19, s[14:15] offset:2688\n"
    "s_waitcnt vmcnt(8)\n"
    ".L_H5:\n"
    "ds_write_b32 v250, v250 offset:176\n"
    "s_add_u32 s56, s57, 12288\n"
    ".L_R2B:\n"
    "s_mov_b32 m0, s41\n"
    "s_add_u32 s41, s41, 4096\n"
    "s_waitcnt vmcnt(6)\n"
    "buffer_load_dwordx4 v2, s[52:55], s57 offen offset:0 sc1 lds\n"
    "buffer_load_dwordx4 v2, s[52:55], s57 offen offset:1024 sc1 lds\n"
    "buffer_load_dwordx4 v2, s[52:55], s57 offen offset:2048 sc1 lds\n"
    "buffer_load_dwordx4 v2, s[52:55], s57 offen offset:3072 sc1 lds\n"
    "s_add_u32 s57, s57, 4096\n"
    "s_cmp_lg_u32 s57, s56\n"
    "s_cbranch_scc1 .L_R2B\n"
    "s_mul_i32 s41, s43, 28672\n"
    "s_cmp_eq_u32 s42, 0\n"
    "s_cbranch_scc1 .L_H6\n"
    "s_waitcnt vmcnt(12)\n"
    "v_perm_b32 v112, v153, v152, s78\n"
    "v_perm_b32 v113, v155, v154, s78\n"
    "v_perm_b32 v114, v157, v156, s78\n"
    "v_perm_b32 v115, v159, v158, s78\n"
    "v_perm_b32 v116, v153, v152, s79\n"
    "v_perm_b32 v117, v155, v154, s79\n"
    "v_perm_b32 v118, v157, v156, s79\n"
    "v_perm_b32 v119, v159, v158, s79\n"
    "buffer_store_dwordx4 v[112:115], v248, s[52:55], s83 offen offset:0 sc1\n"
    "buffer_store_dwordx4 v[116:119], v248, s[52:55], s83 offen offset:128 sc1\n"
    "s_waitcnt vmcnt(2)\n"
    ".L_H7:\n"
    "ds_write_b32 v250, v250 offset:192\n"
    "buffer_load_dword v60, v17, s[52:55], s81 offen sc1\n"
    "s_mov_b32 s60, 1048576\n"
    "ds_read_b32 v252, v250 offset:240\n"
    "s_branch .L_SF1C\n"
    ".L_SF1:\n"
    "s_sleep 6\n"
    "ds_read_b32 v252, v250 offset:240\n"
    ".L_SF1C:\n"
    "s_waitcnt lgkmcnt(0)\n"
    "v_cmp_eq_u32 vcc, 0, v252\n"
    "s_cbranch_vccz .L_SF1OK\n"
    "s_sub_u32 s60, s60, 1\n"
    "s_cbranch_scc0 .L_SF1\n"
    ".L_SF1OK:\n"
    "s_add_u32 s56, s57, 12288\n"
    ".L_R3:\n"
    "s_mov_b32 m0, s41\n"
    "s_add_u32 s41, s41, 4096\n"
    "s_waitcnt vmcnt(6)\n"
    "buffer_load_dwordx4 v2, s[52:55], s57 offen offset:0 sc1 lds\n"
    "buffer_load_dwordx4 v2, s[52:55], s57 offen offset:1024 sc1 lds\n"
    "buffer_load_dwordx4 v2, s[52:55], s57 offen offset:2048 sc1 lds\n"
    "buffer_load_dwordx4 v2, s[52:55], s57 offen offset:3072 sc1 lds\n"
    "s_add_u32 s57, s57, 4096\n"
    "s_cmp_lg_u32 s57, s56\n"
    "s_cbranch_scc1 .L_R3\n"
    "s_mov_b32 s60, 1048576\n"
    "ds_read_b32 v252, v250 offset:256\n"
    "s_branch .L_SF2C\n"
    ".L_SF2:\n"
    "s_sleep 2\n"
    "ds_read_b32 v252, v250 offset:256\n"
    ".L_SF2C:\n"
    "s_waitcnt lgkmcnt(0)\n"
    "v_cmp_eq_u32 vcc, 0, v252\n"
    "s_cbranch_vccz .L_SF2OK\n"
    "s_sub_u32 s60, s60, 1\n"
    "s_cbranch_scc0 .L_SF2\n"
    ".L_SF2OK:\n"
    "s_add_u32 s56, s57, 8192\n"
    ".L_R4:\n"
    "s_mov_b32 m0, s41\n"
    "s_add_u32 s41, s41, 4096\n"
    "s_waitcnt vmcnt(6)\n"
    "buffer_load_dwordx4 v2, s[52:55], s57 offen offset:0 sc1 lds\n"
    "buffer_load_dwordx4 v2, s[52:55], s57 offen offset:1024 sc1 lds\n"
    "buffer_load_dwordx4 v2, s[52:55], s57 offen offset:2048 sc1 lds\n"
    "buffer_load_dwordx4 v2, s[52:55], s57 offen offset:3072 sc1 lds\n"
    "s_add_u32 s57, s57, 4096\n"
    "s_cmp_lg_u32 s57, s56\n"
    "s_cbranch_scc1 .L_R4\n"
    "s_waitcnt vmcnt(8)\n"
    "ds_write_b32 v250, v250 offset:208\n"
    "ds_write_b32 v250, v250 offset:48\n"
    "s_cmp_lg_u32 s32, 4\n"
    "s_cbranch_scc1 .L_F2S\n"
    "s_mov_b32 s60, 1048576\n"
    "ds_read_b32 v252, v251 offset:48\n"
    "s_branch .L_SY3C\n"
    ".L_SY3:\n"
    "s_sleep 1\n"
    "ds_read_b32 v252, v251 offset:48\n"
    ".L_SY3C:\n"
    "s_waitcnt lgkmcnt(0)\n"
    "v_cmp_eq_u32 vcc, 0, v252\n"
    "s_cbranch_vccz .L_SY3OK\n"
    "s_sub_u32 s60, s60, 1\n"
    "s_cbranch_scc0 .L_SY3\n"
    ".L_SY3OK:\n"
    "s_lshl_b32 s58, s2, 7\n"
    "v_mov_b32 v8, s40\n"
    "s_add_u32 s58, s58, 40960\n"
    "s_mov_b64 exec, 1\n"
    "buffer_store_dword v8, off, s[52:55], s58 sc1\n"
    "s_mov_b64 exec, -1\n"
    "s_waitcnt vmcnt(1)\n"
    "s_branch .L_L5\n"
    ".L_F2S:\n"
    "s_waitcnt vmcnt(0)\n"
    ".L_L5:\n"
    "ds_write_b32 v250, v250 offset:224\n"
    "s_mov_b32 s60, 1048576\n"
    "s_branch .L_PWC\n"
    ".L_PW:\n"
    "s_sleep 4\n"
    "buffer_load_dword v60, v17, s[52:55], s81 offen sc1\n"
    "s_waitcnt vmcnt(0)\n"
    ".L_PWC:\n"
    "v_cmp_ne_u32 vcc, s40, v60\n"
    "s_cbranch_vccz .L_PWOK\n"
    "s_sub_u32 s60, s60, 1\n"
    "s_cbranch_scc0 .L_PW\n"
    ".L_PWOK:\n"
    "buffer_load_dword v61, v17, s[52:55], s80 offen sc1\n"
    "buffer_store_dwordx4 v[56:59], v2, s[52:55], s82 offen sc1\n"
    "buffer_store_dwordx4 v[56:59], v2, s[52:55], s82 offen offset:1024 sc1\n"
    "s_waitcnt vmcnt(8)\n"
    "buffer_load_dwordx4 v[120:123], v2, s[52:55], s87 offen offset:0 sc1\n"
    "buffer_load_dwordx4 v[124:127], v2, s[52:55], s87 offen offset:1024 sc1\n"
    "s_waitcnt vmcnt(8)\n"
    "buffer_load_dwordx4 v[128:131], v2, s[52:55], s87 offen offset:2048 sc1\n"
    "buffer_load_dwordx4 v[132:135], v2, s[52:55], s87 offen offset:3072 sc1\n"
    "s_add_u32 s87, s87, 4096\n"
    "s_waitcnt vmcnt(8)\n"
    "buffer_load_dwordx4 v[136:139], v2, s[52:55], s87 offen offset:0 sc1\n"
    "buffer_load_dwordx4 v[140:143], v2, s[52:55], s87 offen offset:1024 sc1\n"
    "s_waitcnt vmcnt(8)\n"
    "buffer_load_dwordx4 v[144:147], v2, s[52:55], s87 offen offset:2048 sc1\n"
    "buffer_load_dwordx4 v[148:151], v2, s[52:55], s87 offen offset:3072 sc1\n"
    "s_add_u32 s87, s87, 4096\n"
    "s_mov_b32 s60, 1048576\n"
    "buffer_load_dword v60, v16, s[52:55], 0 offen sc1\n"
    "s_branch .L_P2C\n"
    ".L_P2:\n"
    "s_sleep 4\n"
    "buffer_load_dword v60, v16, s[52:55], 0 offen sc1\n"
    ".L_P2C:\n"
    "s_waitcnt vmcnt(0)\n"
    "ds_write_b32 v250, v250 offset:64\n"
    "v_cmp_ne_u32 vcc, s40, v60\n"
    "s_cbranch_vccz .L_P2OK\n"
    "s_sub_u32 s60, s60, 1\n"
    "s_cbranch_scc0 .L_P2\n"
    ".L_P2OK:\n"
    "s_cmp_eq_u32 s42, 0\n"
    "s_cbranch_scc1 .L_HN\n"
    "buffer_load_dwordx4 v[104:107], v15, s[52:55], s84 offen sc1\n"
    "buffer_load_dwordx4 v[108:111], v15, s[52:55], s85 offen sc1\n"
    ".L_HN:\n"
    "s_cmp_lg_u32 s32, 4\n"
    "s_cbranch_scc1 .L_F4S\n"
    "s_mov_b32 s60, 1048576\n"
    "ds_read_b32 v252, v251 offset:64\n"
    "s_branch .L_SY4C\n"
    ".L_SY4:\n"
    "s_sleep 1\n"
    "ds_read_b32 v252, v251 offset:64\n"
    ".L_SY4C:\n"
    "s_waitcnt lgkmcnt(0)\n"
    "v_cmp_eq_u32 vcc, 0, v252\n"
    "s_cbranch_vccz .L_SY4OK\n"
    "s_sub_u32 s60, s60, 1\n"
    "s_cbranch_scc0 .L_SY4\n"
    ".L_SY4OK:\n"
    "s_lshl_b32 s58, s2, 7\n"
    "v_mov_b32 v8, s40\n"
    "s_add_u32 s58, s58, 90112\n"
    "s_mov_b64 exec, 1\n"
    "buffer_store_dword v8, off, s[52:55], s58 sc1\n"
    "s_mov_b64 exec, -1\n"
    ".L_F4S:\n"
    "s_waitcnt vmcnt(8)\n"
    "buffer_load_dwordx4 v[152:155], v2, s[52:55], s87 offen offset:0 sc1\n"
    "buffer_load_dwordx4 v[156:159], v2, s[52:55], s87 offen offset:1024 sc1\n"
    "s_waitcnt vmcnt(8)\n"
    "buffer_load_dwordx4 v[160:163], v2, s[52:55], s87 offen offset:2048 sc1\n"
    "buffer_load_dwordx4 v[164:167], v2, s[52:55], s87 offen offset:3072 sc1\n"
    "s_add_u32 s87, s87, 4096\n"
    "s_waitcnt vmcnt(8)\n"
    "buffer_load_dwordx4 v[168:171], v2, s[52:55], s87 offen offset:0 sc1\n"
    "buffer_load_dwordx4 v[172:175], v2, s[52:55], s87 offen offset:1024 sc1\n"
    "s_waitcnt vmcnt(8)\n"
    "buffer_load_dwordx4 v[176:179], v2, s[52:55], s87 offen offset:2048 sc1\n"
    "buffer_load_dwordx4 v[180:183], v2, s[52:55], s87 offen offset:3072 sc1\n"
    "s_add_u32 s87, s87, 4096\n"
    "s_waitcnt vmcnt(8)\n"
    "buffer_load_dwordx4 v[184:187], v2, s[52:55], s87 offen offset:0 sc1\n"
    "buffer_load_dwordx4 v[188:191], v2, s[52:55], s87 offen offset:1024 sc1\n"
    "s_waitcnt vmcnt(8)\n"
    "buffer_load_dwordx4 v[192:195], v2, s[52:55], s87 offen offset:2048 sc1\n"
    "buffer_load_dwordx4 v[196:199], v2, s[52:55], s87 offen offset:3072 sc1\n"
    "s_add_u32 s87, s87, 4096\n"
    "s_waitcnt vmcnt(8)\n"
    "buffer_load_dwordx4 v[200:203], v2, s[52:55], s87 offen offset:0 sc1\n"
    "buffer_load_dwordx4 v[204:207], v2, s[52:55], s87 offen offset:1024 sc1\n"
    "s_waitcnt vmcnt(8)\n"
    "buffer_load_dwordx4 v[208:211], v2, s[52:55], s87 offen offset:2048 sc1\n"
    "buffer_load_dwordx4 v[212:215], v2, s[52:55], s87 offen offset:3072 sc1\n"
    "s_add_u32 s87, s87, 4096\n"
    "s_waitcnt vmcnt(8)\n"
    "buffer_load_dwordx4 v[216:219], v2, s[52:55], s87 offen offset:0 sc1\n"
    "buffer_load_dwordx4 v[220:223], v2, s[52:55], s87 offen offset:1024 sc1\n"
    "s_waitcnt vmcnt(8)\n"
    "buffer_load_dwordx4 v[224:227], v2, s[52:55], s87 offen offset:2048 sc1\n"
    "buffer_load_dwordx4 v[228:231], v2, s[52:55], s87 offen offset:3072 sc1\n"
    "s_add_u32 s87, s87, 4096\n"
    "s_waitcnt vmcnt(8)\n"
    "buffer_load_dwordx4 v[232:235], v2, s[52:55], s87 offen offset:0 sc1\n"
    "buffer_load_dwordx4 v[236:239], v2, s[52:55], s87 offen offset:1024 sc1\n"
    "s_waitcnt vmcnt(8)\n"
    "buffer_load_dwordx4 v[240:243], v2, s[52:55], s87 offen offset:2048 sc1\n"
    "buffer_load_dwordx4 v[244:247], v2, s[52:55], s87 offen offset:3072 sc1\n"
    "s_mov_b32 s60, 1048576\n"
    "buffer_load_dword v60, v23, s[52:55], 0 offen sc1\n"
    "s_branch .L_P4C\n"
    ".L_P4:\n"
    "s_sleep 4\n"
    "buffer_load_dword v60, v23, s[52:55], 0 offen sc1\n"
    ".L_P4C:\n"
    "s_waitcnt vmcnt(0)\n"
    "v_cmp_ne_u32 vcc, s40, v60\n"
    "s_cbranch_vccz .L_P4OK\n"
    "s_sub_u32 s60, s60, 1\n"
    "s_cbranch_scc0 .L_P4\n"
    ".L_P4OK:\n"
    "s_cmp_eq_u32 s42, 0\n"
    "s_cbranch_scc1 .L_HE\n"
    "ds_write2_b64 v14, v[104:105], v[106:107] offset0:0 offset1:32\n"
    "ds_write2_b64 v14, v[108:109], v[110:111] offset0:64 offset1:96\n"
    "ds_write_b32 v250, v250 offset:80\n"
    "s_mov_b32 s60, 1048576\n"
    "ds_read_b32 v252, v251 offset:80\n"
    "s_branch .L_SY5C\n"
    ".L_SY5:\n"
    "s_sleep 1\n"
    "ds_read_b32 v252, v251 offset:80\n"
    ".L_SY5C:\n"
    "s_waitcnt lgkmcnt(0)\n"
    "v_cmp_eq_u32 vcc, 0, v252\n"
    "s_cbranch_vccz .L_SY5OK\n"
    "s_sub_u32 s60, s60, 1\n"
    "s_cbranch_scc0 .L_SY5\n"
    ".L_SY5OK:\n"
    "ds_read_b64 v[40:41], v10 offset:0\n"
    "ds_read_b64 v[42:43], v11 offset:0\n"
    "ds_read_b128 v[56:59], v12 offset:128\n"
    "ds_read_b128 v[60:63], v12 offset:384\n"
    "ds_read_b128 v[64:67], v12 offset:640\n"
    "ds_read_b128 v[68:71], v12 offset:896\n"
    "ds_read_b64 v[44:45], v10 offset:512\n"
    "ds_read_b64 v[46:47], v11 offset:512\n"
    "ds_read_b64 v[48:49], v10 offset:1024\n"
    "ds_read_b64 v[50:51], v11 offset:1024\n"
    "ds_read_b64 v[52:53], v10 offset:1536\n"
    "ds_read_b64 v[54:55], v11 offset:1536\n"
    "s_barrier\n"
    ".L_CONV:\n"
    "s_waitcnt lgkmcnt(6)\n"
    "v_mfma_f32_16x16x32_bf16 v[24:27], v[40:43], v[56:59], v[24:27]\n"
    "ds_read_b64 v[72:73], v10 offset:32\n"
    "ds_read_b64 v[74:75], v11 offset:32\n"
    "v_mfma_f32_16x16x32_bf16 v[28:31], v[40:43], v[60:63], v[28:31]\n"
    "ds_read_b128 v[88:91], v12 offset:112\n"
    "ds_read_b128 v[92:95], v12 offset:368\n"
    "v_mfma_f32_16x16x32_bf16 v[32:35], v[40:43], v[64:67], v[32:35]\n"
    "ds_read_b128 v[96:99], v12 offset:624\n"
    "v_mfma_f32_16x16x32_bf16 v[36:39], v[40:43], v[68:71], v[36:39]\n"
    "ds_read_b128 v[100:103], v12 offset:880\n"
    "s_waitcnt lgkmcnt(6)\n"
    "v_mfma_f32_16x16x32_bf16 v[28:31], v[44:47], v[56:59], v[28:31]\n"
    "ds_read_b64 v[76:77], v10 offset:544\n"
    "v_mfma_f32_16x16x32_bf16 v[32:35], v[44:47], v[60:63], v[32:35]\n"
    "ds_read_b64 v[78:79], v11 offset:544\n"
    "v_mfma_f32_16x16x32_bf16 v[36:39], v[44:47], v[64:67], v[36:39]\n"
    "ds_read_b64 v[80:81], v10 offset:1056\n"
    "v_mfma_f32_16x16x32_bf16 v[32:35], v[48:51], v[56:59], v[32:35]\n"
    "ds_read_b64 v[82:83], v11 offset:1056\n"
    "v_mfma_f32_16x16x32_bf16 v[36:39], v[48:51], v[60:63], v[36:39]\n"
    "ds_read_b64 v[84:85], v10 offset:1568\n"
    "v_mfma_f32_16x16x32_bf16 v[36:39], v[52:55], v[56:59], v[36:39]\n"
    "ds_read_b64 v[86:87], v11 offset:1568\n"
    "s_waitcnt lgkmcnt(6)\n"
    "v_mfma_f32_16x16x32_bf16 v[24:27], v[72:75], v[88:91], v[24:27]\n"
    "ds_read_b64 v[40:41], v10 offset:64\n"
    "ds_read_b64 v[42:43], v11 offset:64\n"
    "v_mfma_f32_16x16x32_bf16 v[28:31], v[72:75], v[92:95], v[28:31]\n"
    "ds_read_b128 v[56:59], v12 offset:96\n"
    "ds_read_b128 v[60:63], v12 offset:352\n"
    "v_mfma_f32_16x16x32_bf16 v[32:35], v[72:75], v[96:99], v[32:35]\n"
    "ds_read_b128 v[64:67], v12 offset:608\n"
    "v_mfma_f32_16x16x32_bf16 v[36:39], v[72:75], v[100:103], v[36:39]\n"
    "ds_read_b128 v[68:71], v12 offset:864\n"
    "s_waitcnt lgkmcnt(6)\n"
    "v_mfma_f32_16x16x32_bf16 v[28:31], v[76:79], v[88:91], v[28:31]\n"
    "ds_read_b64 v[44:45], v10 offset:576\n"
    "v_mfma_f32_16x16x32_bf16 v[32:35], v[76:79], v[92:95], v[32:35]\n"
    "ds_read_b64 v[46:47], v11 offset:576\n"
    "v_mfma_f32_16x16x32_bf16 v[36:39], v[76:79], v[96:99], v[36:39]\n"
    "ds_read_b64 v[48:49], v10 offset:1088\n"
    "v_mfma_f32_16x16x32_bf16 v[32:35], v[80:83], v[88:91], v[32:35]\n"
    "ds_read_b64 v[50:51], v11 offset:1088\n"
    "v_mfma_f32_16x16x32_bf16 v[36:39], v[80:83], v[92:95], v[36:39]\n"
    "ds_read_b64 v[52:53], v10 offset:1600\n"
    "v_mfma_f32_16x16x32_bf16 v[36:39], v[84:87], v[88:91], v[36:39]\n"
    "ds_read_b64 v[54:55], v11 offset:1600\n"
    "s_waitcnt lgkmcnt(6)\n"
    "v_mfma_f32_16x16x32_bf16 v[24:27], v[40:43], v[56:59], v[24:27]\n"
    "ds_read_b64 v[72:73], v10 offset:96\n"
    "ds_read_b64 v[74:75], v11 offset:96\n"
    "v_mfma_f32_16x16x32_bf16 v[28:31], v[40:43], v[60:63], v[28:31]\n"
    "ds_read_b128 v[88:91], v12 offset:80\n"
    "ds_read_b128 v[92:95], v12 offset:336\n"
    "v_mfma_f32_16x16x32_bf16 v[32:35], v[40:43], v[64:67], v[32:35]\n"
    "ds_read_b128 v[96:99], v12 offset:592\n"
    "v_mfma_f32_16x16x32_bf16 v[36:39], v[40:43], v[68:71], v[36:39]\n"
    "ds_read_b128 v[100:103], v12 offset:848\n"
    "s_waitcnt lgkmcnt(6)\n"
    "v_mfma_f32_16x16x32_bf16 v[28:31], v[44:47], v[56:59], v[28:31]\n"
    "ds_read_b64 v[76:77], v10 offset:608\n"
    "v_mfma_f32_16x16x32_bf16 v[32:35], v[44:47], v[60:63], v[32:35]\n"
    "ds_read_b64 v[78:79], v11 offset:608\n"
    "v_mfma_f32_16x16x32_bf16 v[36:39], v[44:47], v[64:67], v[36:39]\n"
    "ds_read_b64 v[80:81], v10 offset:1120\n"
    "v_mfma_f32_16x16x32_bf16 v[32:35], v[48:51], v[56:59], v[32:35]\n"
    "ds_read_b64 v[82:83], v11 offset:1120\n"
    "v_mfma_f32_16x16x32_bf16 v[36:39], v[48:51], v[60:63], v[36:39]\n"
    "ds_read_b64 v[84:85], v10 offset:1632\n"
    "v_mfma_f32_16x16x32_bf16 v[36:39], v[52:55], v[56:59], v[36:39]\n"
    "ds_read_b64 v[86:87], v11 offset:1632\n"
    "s_waitcnt lgkmcnt(6)\n"
    "v_mfma_f32_16x16x32_bf16 v[24:27], v[72:75], v[88:91], v[24:27]\n"
    "ds_read_b64 v[40:41], v10 offset:128\n"
    "ds_read_b64 v[42:43], v11 offset:128\n"
    "v_mfma_f32_16x16x32_bf16 v[28:31], v[72:75], v[92:95], v[28:31]\n"
    "ds_read_b128 v[56:59], v12 offset:64\n"
    "ds_read_b128 v[60:63], v12 offset:320\n"
    "v_mfma_f32_16x16x32_bf16 v[32:35], v[72:75], v[96:99], v[32:35]\n"
    "ds_read_b128 v[64:67], v12 offset:576\n"
    "v_mfma_f32_16x16x32_bf16 v[36:39], v[72:75], v[100:103], v[36:39]\n"
    "ds_read_b128 v[68:71], v12 offset:832\n"
    "s_waitcnt lgkmcnt(6)\n"
    "v_mfma_f32_16x16x32_bf16 v[28:31], v[76:79], v[88:91], v[28:31]\n"
    "ds_read_b64 v[44:45], v10 offset:640\n"
    "v_mfma_f32_16x16x32_bf16 v[32:35], v[76:79], v[92:95], v[32:35]\n"
    "ds_read_b64 v[46:47], v11 offset:640\n"
    "v_mfma_f32_16x16x32_bf16 v[36:39], v[76:79], v[96:99], v[36:39]\n"
    "ds_read_b64 v[48:49], v10 offset:1152\n"
    "v_mfma_f32_16x16x32_bf16 v[32:35], v[80:83], v[88:91], v[32:35]\n"
    "ds_read_b64 v[50:51], v11 offset:1152\n"
    "v_mfma_f32_16x16x32_bf16 v[36:39], v[80:83], v[92:95], v[36:39]\n"
    "ds_read_b64 v[52:53], v10 offset:1664\n"
    "v_mfma_f32_16x16x32_bf16 v[36:39], v[84:87], v[88:91], v[36:39]\n"
    "ds_read_b64 v[54:55], v11 offset:1664\n"
    "s_waitcnt lgkmcnt(6)\n"
    "v_mfma_f32_16x16x32_bf16 v[24:27], v[40:43], v[56:59], v[24:27]\n"
    "ds_read_b64 v[72:73], v10 offset:160\n"
    "ds_read_b64 v[74:75], v11 offset:160\n"
    "v_mfma_f32_16x16x32_bf16 v[28:31], v[40:43], v[60:63], v[28:31]\n"
    "ds_read_b128 v[88:91], v12 offset:48\n"
    "ds_read_b128 v[92:95], v12 offset:304\n"
    "v_mfma_f32_16x16x32_bf16 v[32:35], v[40:43], v[64:67], v[32:35]\n"
    "ds_read_b128 v[96:99], v12 offset:560\n"
    "v_mfma_f32_16x16x32_bf16 v[36:39], v[40:43], v[68:71], v[36:39]\n"
    "ds_read_b128 v[100:103], v12 offset:816\n"
    "s_waitcnt lgkmcnt(6)\n"
    "v_mfma_f32_16x16x32_bf16 v[28:31], v[44:47], v[56:59], v[28:31]\n"
    "ds_read_b64 v[76:77], v10 offset:672\n"
    "v_mfma_f32_16x16x32_bf16 v[32:35], v[44:47], v[60:63], v[32:35]\n"
    "ds_read_b64 v[78:79], v11 offset:672\n"
    "v_mfma_f32_16x16x32_bf16 v[36:39], v[44:47], v[64:67], v[36:39]\n"
    "ds_read_b64 v[80:81], v10 offset:1184\n"
    "v_mfma_f32_16x16x32_bf16 v[32:35], v[48:51], v[56:59], v[32:35]\n"
    "ds_read_b64 v[82:83], v11 offset:1184\n"
    "v_mfma_f32_16x16x32_bf16 v[36:39], v[48:51], v[60:63], v[36:39]\n"
    "ds_read_b64 v[84:85], v10 offset:1696\n"
    "v_mfma_f32_16x16x32_bf16 v[36:39], v[52:55], v[56:59], v[36:39]\n"
    "ds_read_b64 v[86:87], v11 offset:1696\n"
    "s_waitcnt lgkmcnt(6)\n"
    "v_mfma_f32_16x16x32_bf16 v[24:27], v[72:75], v[88:91], v[24:27]\n"
    "ds_read_b64 v[40:41], v10 offset:192\n"
    "ds_read_b64 v[42:43], v11 offset:192\n"
    "v_mfma_f32_16x16x32_bf16 v[28:31], v[72:75], v[92:95], v[28:31]\n"
    "ds_read_b128 v[56:59], v12 offset:32\n"
    "ds_read_b128 v[60:63], v12 offset:288\n"
    "v_mfma_f32_16x16x32_bf16 v[32:35], v[72:75], v[96:99], v[32:35]\n"
    "ds_read_b128 v[64:67], v12 offset:544\n"
    "v_mfma_f32_16x16x32_bf16 v[36:39], v[72:75], v[100:103], v[36:39]\n"
    "ds_read_b128 v[68:71], v12 offset:800\n"
    "s_waitcnt lgkmcnt(6)\n"
    "v_mfma_f32_16x16x32_bf16 v[28:31], v[76:79], v[88:91], v[28:31]\n"
    "ds_read_b64 v[44:45], v10 offset:704\n"
    "v_mfma_f32_16x16x32_bf16 v[32:35], v[76:79], v[92:95], v[32:35]\n"
    "ds_read_b64 v[46:47], v11 offset:704\n"
    "v_mfma_f32_16x16x32_bf16 v[36:39], v[76:79], v[96:99], v[36:39]\n"
    "ds_read_b64 v[48:49], v10 offset:1216\n"
    "v_mfma_f32_16x16x32_bf16 v[32:35], v[80:83], v[88:91], v[32:35]\n"
    "ds_read_b64 v[50:51], v11 offset:1216\n"
    "v_mfma_f32_16x16x32_bf16 v[36:39], v[80:83], v[92:95], v[36:39]\n"
    "ds_read_b64 v[52:53], v10 offset:1728\n"
    "v_mfma_f32_16x16x32_bf16 v[36:39], v[84:87], v[88:91], v[36:39]\n"
    "ds_read_b64 v[54:55], v11 offset:1728\n"
    "s_waitcnt lgkmcnt(6)\n"
    "v_mfma_f32_16x16x32_bf16 v[24:27], v[40:43], v[56:59], v[24:27]\n"
    "ds_read_b64 v[72:73], v10 offset:224\n"
    "ds_read_b64 v[74:75], v11 offset:224\n"
    "v_mfma_f32_16x16x32_bf16 v[28:31], v[40:43], v[60:63], v[28:31]\n"
    "ds_read_b128 v[88:91], v12 offset:16\n"
    "ds_read_b128 v[92:95], v12 offset:272\n"
    "v_mfma_f32_16x16x32_bf16 v[32:35], v[40:43], v[64:67], v[32:35]\n"
    "ds_read_b128 v[96:99], v12 offset:528\n"
    "v_mfma_f32_16x16x32_bf16 v[36:39], v[40:43], v[68:71], v[36:39]\n"
    "ds_read_b128 v[100:103], v12 offset:784\n"
    "s_waitcnt lgkmcnt(6)\n"
    "v_mfma_f32_16x16x32_bf16 v[28:31], v[44:47], v[56:59], v[28:31]\n"
    "ds_read_b64 v[76:77], v10 offset:736\n"
    "v_mfma_f32_16x16x32_bf16 v[32:35], v[44:47], v[60:63], v[32:35]\n"
    "ds_read_b64 v[78:79], v11 offset:736\n"
    "v_mfma_f32_16x16x32_bf16 v[36:39], v[44:47], v[64:67], v[36:39]\n"
    "ds_read_b64 v[80:81], v10 offset:1248\n"
    "v_mfma_f32_16x16x32_bf16 v[32:35], v[48:51], v[56:59], v[32:35]\n"
    "ds_read_b64 v[82:83], v11 offset:1248\n"
    "v_mfma_f32_16x16x32_bf16 v[36:39], v[48:51], v[60:63], v[36:39]\n"
    "ds_read_b64 v[84:85], v10 offset:1760\n"
    "v_mfma_f32_16x16x32_bf16 v[36:39], v[52:55], v[56:59], v[36:39]\n"
    "ds_read_b64 v[86:87], v11 offset:1760\n"
    "s_waitcnt lgkmcnt(6)\n"
    "v_mfma_f32_16x16x32_bf16 v[24:27], v[72:75], v[88:91], v[24:27]\n"
    "ds_read_b64 v[40:41], v10 offset:256\n"
    "ds_read_b64 v[42:43], v11 offset:256\n"
    "v_mfma_f32_16x16x32_bf16 v[28:31], v[72:75], v[92:95], v[28:31]\n"
    "ds_read_b128 v[56:59], v12 offset:0\n"
    "ds_read_b128 v[60:63], v12 offset:256\n"
    "v_mfma_f32_16x16x32_bf16 v[32:35], v[72:75], v[96:99], v[32:35]\n"
    "ds_read_b128 v[64:67], v12 offset:512\n"
    "v_mfma_f32_16x16x32_bf16 v[36:39], v[72:75], v[100:103], v[36:39]\n"
    "ds_read_b128 v[68:71], v12 offset:768\n"
    "s_waitcnt lgkmcnt(6)\n"
    "v_mfma_f32_16x16x32_bf16 v[28:31], v[76:79], v[88:91], v[28:31]\n"
    "ds_read_b64 v[44:45], v10 offset:768\n"
    "v_mfma_f32_16x16x32_bf16 v[32:35], v[76:79], v[92:95], v[32:35]\n"
    "ds_read_b64 v[46:47], v11 offset:768\n"
    "v_mfma_f32_16x16x32_bf16 v[36:39], v[76:79], v[96:99], v[36:39]\n"
    "ds_read_b64 v[48:49], v10 offset:1280\n"
    "v_mfma_f32_16x16x32_bf16 v[32:35], v[80:83], v[88:91], v[32:35]\n"
    "ds_read_b64 v[50:51], v11 offset:1280\n"
    "v_mfma_f32_16x16x32_bf16 v[36:39], v[80:83], v[92:95], v[36:39]\n"
    "ds_read_b64 v[52:53], v10 offset:1792\n"
    "v_mfma_f32_16x16x32_bf16 v[36:39], v[84:87], v[88:91], v[36:39]\n"
    "ds_read_b64 v[54:55], v11 offset:1792\n"
    "v_add_u32 v10, 256, v10\n"
    "v_add_u32 v11, 256, v11\n"
    "v_add_u32 v12, -128, v12\n"
    "s_add_u32 s65, s65, 1\n"
    "s_cmp_lg_u32 s65, 2\n"
    "s_cbranch_scc1 .L_CONV\n"
    "v_cvt_pk_bf16_f32 v72, v24, v25\n"
    "v_cvt_pk_bf16_f32 v73, v26, v27\n"
    "v_cvt_pk_bf16_f32 v74, v28, v29\n"
    "v_cvt_pk_bf16_f32 v75, v30, v31\n"
    "v_min_u32 v72, -2, v72\n"
    "v_min_u32 v73, -2, v73\n"
    "v_min_u32 v74, -2, v74\n"
    "v_min_u32 v75, -2, v75\n"
    "v_xnor_b32 v104, v72, v104\n"
    "v_xnor_b32 v105, v73, v105\n"
    "v_xnor_b32 v106, v74, v106\n"
    "v_xnor_b32 v107, v75, v107\n"
    "buffer_store_dwordx4 v[104:107], v15, s[52:55], s84 offen sc1\n"
    "v_cvt_pk_bf16_f32 v76, v32, v33\n"
    "v_cvt_pk_bf16_f32 v77, v34, v35\n"
    "v_cvt_pk_bf16_f32 v78, v36, v37\n"
    "v_cvt_pk_bf16_f32 v79, v38, v39\n"
    "v_min_u32 v76, -2, v76\n"
    "v_min_u32 v77, -2, v77\n"
    "v_min_u32 v78, -2, v78\n"
    "v_min_u32 v79, -2, v79\n"
    "v_xnor_b32 v108, v76, v108\n"
    "v_xnor_b32 v109, v77, v109\n"
    "v_xnor_b32 v110, v78, v110\n"
    "v_xnor_b32 v111, v79, v111\n"
    "buffer_store_dwordx4 v[108:111], v15, s[52:55], s85 offen sc1\n"
    "ds_write_b128 v22, v[112:115] offset:12288\n"
    "ds_write_b128 v22, v[116:119] offset:13312\n"
    "ds_write_b64 v22, v[20:21] offset:14336\n"
    "s_waitcnt vmcnt(2)\n"
    ".L_HO:\n"
    "s_waitcnt lgkmcnt(0)\n"
    "s_barrier\n"
    "s_cmp_ge_u32 s86, 14\n"
    "s_cbranch_scc1 .L_END\n"
    "v_and_b32 v9, 16, v0\n"
    "v_mul_u32_u24 v9, 1790, v9\n"
    "v_sub_u32 v249, v249, v9\n"
    "v_add_u32 v9, 28672, v249\n"
    "ds_read_b128 v[40:43], v2 offset:4096\n"
    "ds_read_b128 v[44:47], v2 offset:5120\n"
    "ds_read_b128 v[48:51], v2 offset:6144\n"
    "ds_read_b128 v[52:55], v2 offset:7168\n"
    "ds_read_b128 v[56:59], v2 offset:8192\n"
    "ds_read_b128 v[60:63], v2 offset:9216\n"
    "ds_read_b128 v[64:67], v2 offset:10240\n"
    "ds_read_b128 v[68:71], v2 offset:11264\n"
    "s_waitcnt lgkmcnt(7)\n"
    "v_mfma_f32_32x32x16_bf16 v[24:39], v[120:123], v[40:43], 0\n"
    "ds_read_b128 v[72:75], v2 offset:32768\n"
    "s_waitcnt lgkmcnt(7)\n"
    "v_mfma_f32_32x32x16_bf16 v[24:39], v[124:127], v[44:47], v[24:39]\n"
    "ds_read_b128 v[76:79], v2 offset:33792\n"
    "s_waitcnt lgkmcnt(7)\n"
    "v_mfma_f32_32x32x16_bf16 v[24:39], v[128:131], v[48:51], v[24:39]\n"
    "ds_read_b128 v[80:83], v2 offset:34816\n"
    "s_waitcnt lgkmcnt(7)\n"
    "v_mfma_f32_32x32x16_bf16 v[24:39], v[132:135], v[52:55], v[24:39]\n"
    "ds_read_b128 v[84:87], v2 offset:35840\n"
    "s_waitcnt lgkmcnt(7)\n"
    "v_mfma_f32_32x32x16_bf16 v[24:39], v[136:139], v[56:59], v[24:39]\n"
    "ds_read_b128 v[88:91], v2 offset:36864\n"
    "s_waitcnt lgkmcnt(7)\n"
    "v_mfma_f32_32x32x16_bf16 v[24:39], v[140:143], v[60:63], v[24:39]\n"
    "ds_read_b128 v[92:95], v2 offset:37888\n"
    "s_waitcnt lgkmcnt(7)\n"
    "v_mfma_f32_32x32x16_bf16 v[24:39], v[144:147], v[64:67], v[24:39]\n"
    "ds_read_b128 v[96:99], v2 offset:38912\n"
    "s_waitcnt lgkmcnt(7)\n"
    "v_mfma_f32_32x32x16_bf16 v[24:39], v[148:151], v[68:71], v[24:39]\n"
    "ds_read_b128 v[100:103], v2 offset:39936\n"
    "s_waitcnt lgkmcnt(7)\n"
    "v_mfma_f32_32x32x16_bf16 v[24:39], v[152:155], v[72:75], v[24:39]\n"
    "ds_read_b128 v[40:43], v13 offset:4096\n"
    "s_waitcnt lgkmcnt(7)\n"
    "v_mfma_f32_32x32x16_bf16 v[24:39], v[156:159], v[76:79], v[24:39]\n"
    "ds_read_b128 v[44:47], v13 offset:5120\n"
    "s_waitcnt lgkmcnt(7)\n"
    "v_mfma_f32_32x32x16_bf16 v[24:39], v[160:163], v[80:83], v[24:39]\n"
    "ds_read_b128 v[48:51], v13 offset:6144\n"
    "s_waitcnt lgkmcnt(7)\n"
    "v_mfma_f32_32x32x16_bf16 v[24:39], v[164:167], v[84:87], v[24:39]\n"
    "ds_read_b128 v[52:55], v13 offset:7168\n"
    "s_waitcnt lgkmcnt(7)\n"
    "v_mfma_f32_32x32x16_bf16 v[24:39], v[168:171], v[88:91], v[24:39]\n"
    "ds_read_b128 v[56:59], v13 offset:8192\n"
    "s_waitcnt lgkmcnt(7)\n"
    "v_mfma_f32_32x32x16_bf16 v[24:39], v[172:175], v[92:95], v[24:39]\n"
    "ds_read_b128 v[60:63], v13 offset:9216\n"
    "s_waitcnt lgkmcnt(7)\n"
    "v_mfma_f32_32x32x16_bf16 v[24:39], v[176:179], v[96:99], v[24:39]\n"
    "ds_read_b128 v[64:67], v13 offset:10240\n"
    "s_waitcnt lgkmcnt(7)\n"
    "v_mfma_f32_32x32x16_bf16 v[24:39], v[180:183], v[100:103], v[24:39]\n"
    "ds_read_b128 v[68:71], v13 offset:11264\n"
    "s_waitcnt lgkmcnt(7)\n"
    "v_mfma_f32_32x32x16_bf16 v[24:39], v[184:187], v[40:43], v[24:39]\n"
    "ds_read_b128 v[72:75], v13 offset:32768\n"
    "s_waitcnt lgkmcnt(7)\n"
    "v_mfma_f32_32x32x16_bf16 v[24:39], v[188:191], v[44:47], v[24:39]\n"
    "ds_read_b128 v[76:79], v13 offset:33792\n"
    "s_waitcnt lgkmcnt(7)\n"
    "v_mfma_f32_32x32x16_bf16 v[24:39], v[192:195], v[48:51], v[24:39]\n"
    "ds_read_b128 v[80:83], v13 offset:34816\n"
    "s_waitcnt lgkmcnt(7)\n"
    "v_mfma_f32_32x32x16_bf16 v[24:39], v[196:199], v[52:55], v[24:39]\n"
    "ds_read_b128 v[84:87], v13 offset:35840\n"
    "s_waitcnt lgkmcnt(7)\n"
    "v_mfma_f32_32x32x16_bf16 v[24:39], v[200:203], v[56:59], v[24:39]\n"
    "ds_read_b128 v[88:91], v13 offset:36864\n"
    "s_waitcnt lgkmcnt(7)\n"
    "v_mfma_f32_32x32x16_bf16 v[24:39], v[204:207], v[60:63], v[24:39]\n"
    "ds_read_b128 v[92:95], v13 offset:37888\n"
    "s_waitcnt lgkmcnt(7)\n"
    "v_mfma_f32_32x32x16_bf16 v[24:39], v[208:211], v[64:67], v[24:39]\n"
    "ds_read_b128 v[96:99], v13 offset:38912\n"
    "s_waitcnt lgkmcnt(7)\n"
    "v_mfma_f32_32x32x16_bf16 v[24:39], v[212:215], v[68:71], v[24:39]\n"
    "ds_read_b128 v[100:103], v13 offset:39936\n"
    "s_waitcnt lgkmcnt(7)\n"
    "v_mfma_f32_32x32x16_bf16 v[24:39], v[216:219], v[72:75], v[24:39]\n"
    "s_waitcnt lgkmcnt(6)\n"
    "v_mfma_f32_32x32x16_bf16 v[24:39], v[220:223], v[76:79], v[24:39]\n"
    "s_waitcnt lgkmcnt(5)\n"
    "v_mfma_f32_32x32x16_bf16 v[24:39], v[224:227], v[80:83], v[24:39]\n"
    "s_waitcnt lgkmcnt(4)\n"
    "v_mfma_f32_32x32x16_bf16 v[24:39], v[228:231], v[84:87], v[24:39]\n"
    "s_waitcnt lgkmcnt(3)\n"
    "v_mfma_f32_32x32x16_bf16 v[24:39], v[232:235], v[88:91], v[24:39]\n"
    "s_waitcnt lgkmcnt(2)\n"
    "v_mfma_f32_32x32x16_bf16 v[24:39], v[236:239], v[92:95], v[24:39]\n"
    "s_waitcnt lgkmcnt(1)\n"
    "v_mfma_f32_32x32x16_bf16 v[24:39], v[240:243], v[96:99], v[24:39]\n"
    "s_waitcnt lgkmcnt(0)\n"
    "v_mfma_f32_32x32x16_bf16 v[24:39], v[244:247], v[100:103], v[24:39]\n"
    "s_nop 11\n"
    "v_cvt_pk_bf16_f32 v40, v24, v25\n"
    "v_cvt_pk_bf16_f32 v44, v32, v33\n"
    "v_cvt_pk_bf16_f32 v41, v26, v27\n"
    "v_cvt_pk_bf16_f32 v45, v34, v35\n"
    "v_cvt_pk_bf16_f32 v42, v28, v29\n"
    "v_cvt_pk_bf16_f32 v46, v36, v37\n"
    "v_cvt_pk_bf16_f32 v43, v30, v31\n"
    "v_cvt_pk_bf16_f32 v47, v38, v39\n"
    "v_permlane16_swap_b32 v40, v44\n"
    "v_permlane16_swap_b32 v41, v45\n"
    "v_permlane16_swap_b32 v42, v46\n"
    "v_permlane16_swap_b32 v43, v47\n"
    "global_store_dwordx4 v249, v[40:43], s[12:13] sc1\n"
    "global_store_dwordx4 v9, v[44:47], s[12:13] sc1\n"
    "s_endpgm\n"
    ".L_H4:\n"
    "s_waitcnt vmcnt(0)\n"
    "s_branch .L_H5\n"
    ".L_H6:\n"
    "s_waitcnt vmcnt(0)\n"
    "s_branch .L_H7\n"
    ".L_HE:\n"
    "s_or_b32 s58, s2, s43\n"
    "s_cbranch_scc1 .L_HO5\n"
    "v_lshlrev_b32 v16, 9, v1\n"
    "s_mov_b32 s58, 40960\n"
    "s_mov_b32 s60, 1048576\n"
    "buffer_load_dword v60, v16, s[52:55], s58 offen offset:0 sc1\n"
    "buffer_load_dword v61, v16, s[52:55], s58 offen offset:128 sc1\n"
    "buffer_load_dword v62, v16, s[52:55], s58 offen offset:256 sc1\n"
    "buffer_load_dword v63, v16, s[52:55], s58 offen offset:384 sc1\n"
    "s_branch .L_P5C\n"
    ".L_P5:\n"
    "s_sleep 4\n"
    "buffer_load_dword v60, v16, s[52:55], s58 offen offset:0 sc1\n"
    "buffer_load_dword v61, v16, s[52:55], s58 offen offset:128 sc1\n"
    "buffer_load_dword v62, v16, s[52:55], s58 offen offset:256 sc1\n"
    "buffer_load_dword v63, v16, s[52:55], s58 offen offset:384 sc1\n"
    ".L_P5C:\n"
    "s_waitcnt vmcnt(0)\n"
    "v_bitop3_b32 v60, v60, v61, s40 bitop3:0x7e\n"
    "v_bitop3_b32 v62, v62, v63, s40 bitop3:0x7e\n"
    "v_or_b32 v60, v60, v62\n"
    "v_cmp_ne_u32 vcc, 0, v60\n"
    "s_cbranch_vccz .L_P5OK\n"
    "s_sub_u32 s60, s60, 1\n"
    "s_cbranch_scc0 .L_P5\n"
    ".L_P5OK:\n"
    "s_mov_b64 exec, 1\n"
    "buffer_store_dword v8, off, s[52:55], 0 sc1\n"
    "s_mov_b64 exec, -1\n"
    ".L_HO5:\n"
    "s_barrier\n"
    "s_branch .L_HO\n"
    ".L_KVA:\n"
    "s_lshl2_add_u32 s62, s35, s32\n"
    "s_lshl_b32 s62, s62, 11\n"
    "s_add_u32 s62, s62, 131072\n"
    "v_mov_b32 v240, v2\n"
    "s_mov_b32 s67, 0\n"
    "s_waitcnt lgkmcnt(0)\n"
    "s_mul_i32 s40, s40, 0x19660d\n"
    "s_add_u32 s40, s40, 0x3c6ef35f\n"
    "s_cmp_eq_u32 s64, 0\n"
    "s_cbranch_scc1 .L_GQ\n"
    "v_mul_u32_u24 v8, 37, v3\n"
    "v_lshrrev_b32 v8, 8, v8\n"
    "v_mul_u32_u24 v9, 7, v8\n"
    "v_sub_u32 v9, v3, v9\n"
    "v_cmp_lt_u32 vcc, 27, v3\n"
    "v_mov_b32 v24, 0x3f803f80\n"
    "v_cmp_gt_u32 s[58:59], 30, v3\n"
    "s_mov_b32 s66, 0x5d800000\n"
    "v_cndmask_b32 v8, v8, v9, vcc\n"
    "v_cndmask_b32 v9, v9, 7, vcc\n"
    "v_and_b32 v7, 4, v9\n"
    "v_add_u32 v9, v7, v9\n"
    "v_and_b32 v7, 2, v8\n"
    "v_add_u32 v8, v7, v8\n"
    "v_lshl_add_u32 v7, v8, 2, v9\n"
    "v_lshl_add_u32 v7, v4, 5, v7\n"
    "v_lshlrev_b32 v240, 4, v7\n"
    ".L_GV:\n"
    "v_add_u32 v46, s41, v44\n"
    "v_add_u32 v50, s41, v45\n"
    "v_xor_b32 v47, 32, v46\n"
    "v_xor_b32 v48, 64, v46\n"
    "v_xor_b32 v49, 0x60, v46\n"
    "s_add_u32 s41, s41, 8192\n"
    "s_cmp_eq_u32 s41, 57344\n"
    "s_cselect_b32 s41, 0, s41\n"
    "s_add_u32 s65, s65, 1\n"
    "s_cmp_lg_u32 s65, 7\n"
    "s_barrier\n"
    "ds_read_b128 v[128:131], v46\n"
    "ds_read_b64_tr_b16 v[132:133], v50 offset:0\n"
    "ds_read_b64_tr_b16 v[134:135], v50 offset:512\n"
    "s_waitcnt lgkmcnt(6)\n"
    "v_mfma_f32_32x32x16_bf16 v[224:239], v[144:147], v[148:151], v[224:239]\n"
    "ds_read_b128 v[136:139], v47\n"
    "ds_read_b64_tr_b16 v[140:141], v50 offset:2048\n"
    "ds_read_b64_tr_b16 v[142:143], v50 offset:2560\n"
    "ds_read_b128 v[144:147], v48\n"
    "ds_read_b64_tr_b16 v[148:149], v50 offset:4096\n"
    "ds_read_b64_tr_b16 v[150:151], v50 offset:4608\n"
    "s_waitcnt lgkmcnt(9)\n"
    "v_mfma_f32_32x32x16_bf16 v[224:239], v[152:155], v[156:159], v[224:239]\n"
    "ds_read_b128 v[152:155], v49\n"
    "ds_read_b64_tr_b16 v[156:157], v50 offset:6144\n"
    "ds_read_b64_tr_b16 v[158:159], v50 offset:6656\n"
    "s_waitcnt lgkmcnt(9)\n"
    "v_mfma_f32_32x32x16_bf16 v[224:239], v[128:131], v[132:135], v[224:239]\n"
    "s_waitcnt lgkmcnt(6)\n"
    "v_mfma_f32_32x32x16_bf16 v[224:239], v[136:139], v[140:143], v[224:239]\n"
    "s_cbranch_scc1 .L_GV\n"
    "s_waitcnt lgkmcnt(3)\n"
    "v_mfma_f32_32x32x16_bf16 v[224:239], v[144:147], v[148:151], v[224:239]\n"
    "s_waitcnt lgkmcnt(0)\n"
    "v_mfma_f32_32x32x16_bf16 v[224:239], v[152:155], v[156:159], v[224:239]\n"
    "s_barrier\n"
    "ds_write_b32 v59, v16 offset:48128\n"
    "s_nop 9\n"
    "v_max_f32 v7, |v224|, |v225|\n"
    "v_max3_f32 v7, |v226|, |v227|, v7\n"
    "v_max3_f32 v7, |v228|, |v229|, v7\n"
    "v_max3_f32 v7, |v230|, |v231|, v7\n"
    "v_max3_f32 v7, |v232|, |v233|, v7\n"
    "v_max3_f32 v7, |v234|, |v235|, v7\n"
    "v_max3_f32 v7, |v236|, |v237|, v7\n"
    "v_max3_f32 v7, |v238|, |v239|, v7\n"
    "v_cmp_le_f32 s[60:61], s66, v7\n"
    "v_cvt_pk_bf16_f32 v16, v224, v225\n"
    "v_cvt_pk_bf16_f32 v17, v226, v227\n"
    "v_cvt_pk_bf16_f32 v18, v228, v229\n"
    "v_cvt_pk_bf16_f32 v19, v230, v231\n"
    "v_cvt_pk_bf16_f32 v20, v232, v233\n"
    "v_cvt_pk_bf16_f32 v21, v234, v235\n"
    "v_cvt_pk_bf16_f32 v22, v236, v237\n"
    "v_cvt_pk_bf16_f32 v23, v238, v239\n"
    "s_cmp_lg_u64 s[60:61], 0\n"
    "s_cselect_b32 s60, 0x7f807f80, 0\n"
    "v_mov_b32 v25, s60\n"
    "v_cndmask_b32 v24, v25, v24, s[58:59]\n"
    "v_cndmask_b32 v16, v16, v24, vcc\n"
    "v_cndmask_b32 v17, v17, v24, vcc\n"
    "v_cndmask_b32 v18, v18, v24, vcc\n"
    "v_cndmask_b32 v19, v19, v24, vcc\n"
    "v_cndmask_b32 v20, v20, v24, vcc\n"
    "v_cndmask_b32 v21, v21, v24, vcc\n"
    "v_cndmask_b32 v22, v22, v24, vcc\n"
    "v_cndmask_b32 v23, v23, v24, vcc\n"
    "s_branch .L_PUB\n"
    ".L_EK:\n"
    "s_mov_b32 vcc_lo, -1\n"
    "s_mov_b32 vcc_hi, 0\n"
    "s_nop 5\n"
    "v_cvt_pk_bf16_f32 v16, v224, v225\n"
    "v_cvt_pk_bf16_f32 v17, v226, v227\n"
    "v_cvt_pk_bf16_f32 v18, v228, v229\n"
    "v_cvt_pk_bf16_f32 v19, v230, v231\n"
    "v_cvt_pk_bf16_f32 v20, v232, v233\n"
    "v_cvt_pk_bf16_f32 v21, v234, v235\n"
    "v_cvt_pk_bf16_f32 v22, v236, v237\n"
    "v_cvt_pk_bf16_f32 v23, v238, v239\n"
    "v_cndmask_b32 v22, 0, v22, vcc\n"
    "v_cndmask_b32 v23, 0, v23, vcc\n"
    ".L_PUB:\n"
    "buffer_store_dwordx4 v[16:19], v240, s[52:55], s62 offen sc1\n"
    "buffer_store_dwordx4 v[20:23], v240, s[52:55], s62 offen offset:1024 sc1\n"
    "s_waitcnt vmcnt(0)\n"
    "s_barrier\n"
    "s_sleep 10\n"
    "buffer_load_dword v16, v13, s[52:55], 0 offen sc1\n"
    "s_branch .L_AP\n"
    ".L_HWL:\n"
    "s_waitcnt vmcnt(8)\n"
    "s_barrier\n"
    "s_waitcnt vmcnt(4)\n"
    "s_barrier\n"
    "s_waitcnt vmcnt(0)\n"
    "s_barrier\n"
    "s_branch .L_HT2\n"
    ".L_HK:\n"
    "s_mov_b64 s[48:49], s[28:29]\n"
    "s_mov_b32 s50, 401408\n"
    "s_mov_b32 s51, 0x20000\n"
    "s_mul_i32 s59, s35, 12544\n"
    "s_mul_i32 s58, s43, 3072\n"
    "s_add_u32 s59, s59, s58\n"
    "s_add_u32 m0, s58, 114688\n"
    "v_and_b32 v7, 1, v0\n"
    "v_bfe_u32 v8, v0, 1, 1\n"
    "v_lshl_add_u32 v7, v7, 1, v8\n"
    "v_and_b32 v8, 16, v0\n"
    "v_lshlrev_b32 v8, 1, v8\n"
    "v_lshl_add_u32 v7, v7, 3, v8\n"
    "s_lshl_b32 s58, s43, 6\n"
    "s_add_u32 s58, s58, 114688\n"
    "v_add_u32 v7, s58, v7\n"
    "v_bfe_u32 v8, v0, 2, 2\n"
    "v_add_u32 v9, 4, v8\n"
    "v_min_u32 v9, 6, v9\n"
    "v_mad_u32_u24 v8, v4, 7, v8\n"
    "v_mad_u32_u24 v9, v4, 7, v9\n"
    "s_movk_i32 s58, 896\n"
    "v_mad_u32_u24 v8, v8, s58, v7\n"
    "v_mad_u32_u24 v9, v9, s58, v7\n"
    "s_lshl_b32 s60, s2, 7\n"
    "s_add_u32 s60, s60, 4096\n"
    "v_mov_b32 v60, s40\n"
    "s_cmp_lg_u32 s32, 4\n"
    "s_barrier\n"
    "s_cbranch_scc1 .L_FP1\n"
    "s_mov_b64 exec, 1\n"
    "buffer_store_dword v60, off, s[52:55], s60 sc1\n"
    "s_mov_b64 exec, -1\n"
    ".L_FP1:\n"
    "s_sleep 4\n"
    "buffer_load_dwordx4 v2, s[48:51], s59 offen offset:0 lds\n"
    "buffer_load_dwordx4 v2, s[48:51], s59 offen offset:1024 lds\n"
    "buffer_load_dwordx4 v2, s[48:51], s59 offen offset:2048 lds\n"
    "buffer_load_dwordx4 v2, s[48:51], s59 offen offset:3072 lds\n"
    "s_waitcnt vmcnt(0)\n"
    "ds_write_b32 v250, v250 offset:16\n"
    "s_mov_b32 s60, 1048576\n"
    "ds_read_b32 v252, v251 offset:16\n"
    "s_branch .L_SK1C\n"
    ".L_SK1:\n"
    "s_sleep 1\n"
    "ds_read_b32 v252, v251 offset:16\n"
    ".L_SK1C:\n"
    "s_waitcnt lgkmcnt(0)\n"
    "v_cmp_eq_u32 vcc, 0, v252\n"
    "s_cbranch_vccz .L_SK1OK\n"
    "s_sub_u32 s60, s60, 1\n"
    "s_cbranch_scc0 .L_SK1\n"
    ".L_SK1OK:\n"
    "ds_read_b64_tr_b16 v[40:41], v8 offset:0\n"
    "ds_read_b64_tr_b16 v[42:43], v9 offset:0\n"
    "ds_read_b64_tr_b16 v[44:45], v8 offset:256\n"
    "ds_read_b64_tr_b16 v[46:47], v9 offset:256\n"
    "ds_read_b64_tr_b16 v[48:49], v8 offset:512\n"
    "ds_read_b64_tr_b16 v[50:51], v9 offset:512\n"
    "ds_read_b64_tr_b16 v[52:53], v8 offset:768\n"
    "ds_read_b64_tr_b16 v[54:55], v9 offset:768\n"
    "s_lshl_b32 s58, s43, 5\n"
    "s_add_u32 s58, s58, s35\n"
    "s_lshl_b32 s58, s58, 10\n"
    "s_add_u32 s58, s58, 524288\n"
    "s_waitcnt lgkmcnt(0)\n"
    "buffer_store_dwordx4 v[40:43], v2, s[52:55], s58 offen sc1\n"
    "s_add_u32 s58, s58, 131072\n"
    "buffer_store_dwordx4 v[44:47], v2, s[52:55], s58 offen sc1\n"
    "s_add_u32 s58, s58, 131072\n"
    "buffer_store_dwordx4 v[48:51], v2, s[52:55], s58 offen sc1\n"
    "s_add_u32 s58, s58, 131072\n"
    "buffer_store_dwordx4 v[52:55], v2, s[52:55], s58 offen sc1\n"
    "s_add_u32 s58, s58, 131072\n"
    "s_waitcnt vmcnt(0)\n"
    "ds_write_b32 v250, v250 offset:32\n"
    "s_mov_b32 s60, 1048576\n"
    "ds_read_b32 v252, v251 offset:32\n"
    "s_branch .L_SK2C\n"
    ".L_SK2:\n"
    "s_sleep 1\n"
    "ds_read_b32 v252, v251 offset:32\n"
    ".L_SK2C:\n"
    "s_waitcnt lgkmcnt(0)\n"
    "v_cmp_eq_u32 vcc, 0, v252\n"
    "s_cbranch_vccz .L_SK2OK\n"
    "s_sub_u32 s60, s60, 1\n"
    "s_cbranch_scc0 .L_SK2\n"
    ".L_SK2OK:\n"
    "s_cmp_lg_u32 s32, 4\n"
    "s_cbranch_scc1 .L_F3S\n"
    "s_lshl_b32 s58, s2, 7\n"
    "v_mov_b32 v8, s40\n"
    "s_add_u32 s58, s58, 77824\n"
    "s_mov_b64 exec, 1\n"
    "buffer_store_dword v8, off, s[52:55], s58 sc1\n"
    "s_mov_b64 exec, -1\n"
    ".L_F3S:\n"
    "s_branch .L_H3K\n"
    ".L_FALL:\n"
    "v_mov_b32 v8, 0xff800000\n"
    "s_mov_b32 s61, 131072\n"
    ".L_F1:\n"
    "buffer_load_dwordx4 v[32:35], v2, s[52:55], s61 offen sc1\n"
    "buffer_load_dwordx4 v[36:39], v2, s[52:55], s61 offen offset:1024 sc1\n"
    "s_add_u32 s61, s61, 4096\n"
    "s_cmp_lg_u32 s61, 393216\n"
    "s_waitcnt vmcnt(0)\n"
    "v_mfma_f32_32x32x16_bf16 v[16:31], v[32:35], v[152:155], 0\n"
    "v_mfma_f32_32x32x16_bf16 v[16:31], v[36:39], v[156:159], v[16:31]\n"
    "s_nop 11\n"
    "v_max3_f32 v8, v8, v16, v17\n"
    "v_max3_f32 v8, v8, v18, v19\n"
    "v_max3_f32 v8, v8, v20, v21\n"
    "v_max3_f32 v8, v8, v22, v23\n"
    "v_max3_f32 v8, v8, v24, v25\n"
    "v_max3_f32 v8, v8, v26, v27\n"
    "v_max3_f32 v8, v8, v28, v29\n"
    "v_max3_f32 v8, v8, v30, v31\n"
    "s_cbranch_scc1 .L_F1\n"
    "v_mov_b32 v9, v8\n"
    "s_nop 1\n"
    "v_permlane32_swap_b32 v8, v9\n"
    "v_max_f32 v8, v8, v9\n"
    "s_mov_b32 s61, 131072\n"
    "v_mov_b64 v[112:113], 0\n"
    "v_mov_b64 v[114:115], 0\n"
    "v_mov_b64 v[116:117], 0\n"
    "v_mov_b64 v[118:119], 0\n"
    "v_mov_b64 v[120:121], 0\n"
    "v_mov_b64 v[122:123], 0\n"
    "v_mov_b64 v[124:125], 0\n"
    "v_mov_b64 v[126:127], 0\n"
    ".L_F2:\n"
    "buffer_load_dwordx4 v[32:35], v2, s[52:55], s61 offen offset:0 sc1\n"
    "buffer_load_dwordx4 v[36:39], v2, s[52:55], s61 offen offset:1024 sc1\n"
    "buffer_load_dwordx4 v[40:43], v2, s[52:55], s61 offen offset:2048 sc1\n"
    "buffer_load_dwordx4 v[44:47], v2, s[52:55], s61 offen offset:3072 sc1\n"
    "s_add_u32 s61, s61, 4096\n"
    "s_cmp_lg_u32 s61, 393216\n"
    "s_waitcnt vmcnt(0)\n"
    "v_mfma_f32_32x32x16_bf16 v[16:31], v[32:35], v[152:155], 0\n"
    "v_mfma_f32_32x32x16_bf16 v[16:31], v[36:39], v[156:159], v[16:31]\n"
    "s_nop 11\n"
    "v_sub_f32 v16, v16, v8\n"
    "v_sub_f32 v17, v17, v8\n"
    "v_sub_f32 v18, v18, v8\n"
    "v_sub_f32 v19, v19, v8\n"
    "v_sub_f32 v20, v20, v8\n"
    "v_sub_f32 v21, v21, v8\n"
    "v_sub_f32 v22, v22, v8\n"
    "v_sub_f32 v23, v23, v8\n"
    "v_sub_f32 v24, v24, v8\n"
    "v_sub_f32 v25, v25, v8\n"
    "v_sub_f32 v26, v26, v8\n"
    "v_sub_f32 v27, v27, v8\n"
    "v_sub_f32 v28, v28, v8\n"
    "v_sub_f32 v29, v29, v8\n"
    "v_sub_f32 v30, v30, v8\n"
    "v_sub_f32 v31, v31, v8\n"
    "v_exp_f32 v16, v16\n"
    "v_exp_f32 v17, v17\n"
    "v_exp_f32 v18, v18\n"
    "v_exp_f32 v19, v19\n"
    "v_exp_f32 v20, v20\n"
    "v_exp_f32 v21, v21\n"
    "v_exp_f32 v22, v22\n"
    "v_exp_f32 v23, v23\n"
    "v_exp_f32 v24, v24\n"
    "v_exp_f32 v25, v25\n"
    "v_exp_f32 v26, v26\n"
    "v_exp_f32 v27, v27\n"
    "v_exp_f32 v28, v28\n"
    "v_exp_f32 v29, v29\n"
    "v_exp_f32 v30, v30\n"
    "v_exp_f32 v31, v31\n"
    "v_smfmac_f32_32x32x32_bf16 v[112:127], v[40:43], v[16:23], v10\n"
    "s_nop 0\n"
    "v_smfmac_f32_32x32x32_bf16 v[112:127], v[44:47], v[24:31], v10\n"
    "s_cbranch_scc1 .L_F2\n"
    "s_nop 11\n"
    ".L_LAST:\n"
    "s_branch .L_NORM\n"
    :
    : "{s[0:1]}"((u64)__builtin_amdgcn_kernarg_segment_ptr()), "{s2}"(__builtin_amdgcn_workgroup_id_x()), "{v0}"(__builtin_amdgcn_workitem_id_x())
    : "v1", "v2", "v3", "v4", "v5", "v6", "v7", "v8", "v9", "v10", "v11", "v12", "v13", "v14", "v15", "v16", "v17", "v18",
      "v19", "v20", "v21", "v22", "v23", "v24", "v25", "v26", "v27", "v28", "v29", "v30", "v31", "v32", "v33", "v34", "v35",
      "v36", "v37", "v38", "v39", "v40", "v41", "v42", "v43", "v44", "v45", "v46", "v47", "v48", "v49", "v50", "v51", "v52",
      "v53", "v54", "v55", "v56", "v57", "v58", "v59", "v60", "v61", "v62", "v63", "v64", "v65", "v66", "v67", "v68", "v69",
      "v70", "v71", "v72", "v73", "v74", "v75", "v76", "v77", "v78", "v79", "v80", "v81", "v82", "v83", "v84", "v85", "v86",
      "v87", "v88", "v89", "v90", "v91", "v92", "v93", "v94", "v95", "v96", "v97", "v98", "v99", "v100", "v101", "v102",
      "v103", "v104", "v105", "v106", "v107", "v108", "v109", "v110", "v111", "v112", "v113", "v114", "v115", "v116", "v117",
      "v118", "v119", "v120", "v121", "v122", "v123", "v124", "v125", "v126", "v127", "v128", "v129", "v130", "v131", "v132",
      "v133", "v134", "v135", "v136", "v137", "v138", "v139", "v140", "v141", "v142", "v143", "v144", "v145", "v146", "v147",
      "v148", "v149", "v150", "v151", "v152", "v153", "v154", "v155", "v156", "v157", "v158", "v159", "v160", "v161", "v162",
      "v163", "v164", "v165", "v166", "v167", "v168", "v169", "v170", "v171", "v172", "v173", "v174", "v175", "v176", "v177",
      "v178", "v179", "v180", "v181", "v182", "v183", "v184", "v185", "v186", "v187", "v188", "v189", "v190", "v191", "v192",
      "v193", "v194", "v195", "v196", "v197", "v198", "v199", "v200", "v201", "v202", "v203", "v204", "v205", "v206", "v207",
      "v208", "v209", "v210", "v211", "v212", "v213", "v214", "v215", "v216", "v217", "v218", "v219", "v220", "v221", "v222",
      "v223", "v224", "v225", "v226", "v227", "v228", "v229", "v230", "v231", "v232", "v233", "v234", "v235", "v236", "v237",
      "v238", "v239", "v240", "v241", "v242", "v243", "v244", "v245", "v246", "v247", "v248", "v249", "v250", "v251", "v252",
      "v253", "v254", "v255", "s3", "s4", "s5", "s6", "s7", "s8", "s9", "s10", "s11", "s12", "s13", "s14", "s15", "s16",
      "s17", "s18", "s19", "s20", "s21", "s22", "s23", "s24", "s25", "s26", "s27", "s28", "s29", "s30", "s31", "s33", "s34",
      "s35", "s36", "s37", "s38", "s39", "s40", "s41", "s42", "s43", "s44", "s45", "s46", "s47", "s48", "s49", "s50", "s51",
      "s52", "s53", "s54", "s55", "s56", "s57", "s58", "s59", "s60", "s61", "s62", "s63", "s64", "s65", "s66", "s67", "s68",
      "s69", "s70", "s71", "s72", "s73", "s74", "s75", "s76", "s77", "s78", "s79", "s80", "s81", "s82", "s83", "s84", "s85",
      "s86", "s87", "s88", "s89", "s90", "s91", "s92", "s93", "s94", "s95", "s96", "s97", "s98", "s99", "vcc", "scc", "memory");
#endif
}

extern "C" void kernel_launch(void* const* d_in, const int* in_sizes, int n_in, void* d_out, int out_size, void* d_ws, size_t ws_size,
                              hipStream_t stream) {
    fused_kernel<<<256, 512, 0, stream>>>(d_ws, d_in[0], d_in[1], d_in[2], d_in[3], d_out, d_in[5], d_in[6], d_in[7], d_in[8], d_in[9],
                                          d_in[10], d_in[11], d_in[4]);
}
```
